```python
import torch

def _softmax(x, axis=-1):
    return torch.softmax(x, dim=axis)
def _gelu(x):
    return torch.nn.functional.gelu(x, approximate="tanh")
def _silu(x):
    return torch.nn.functional.silu(x)
def _sigmoid(x):
    return torch.sigmoid(x)
def _layernorm(x, axis=-1, eps=1e-5):
    m = x.mean(dim=axis, keepdim=True)
    v = x.var(dim=axis, keepdim=True, unbiased=False)
    return (x - m) / torch.sqrt(v + eps)
def _scatter_add(tgt, idx, src, axis=0):
    return tgt.clone().index_add_(axis, idx, src)
def _segment_sum(src, seg, n):
    out = torch.zeros((n,) + src.shape[1:], dtype=src.dtype)
    return out.index_add_(0, seg, src)
def _pad(x, pw):
    flat = tuple(v for pair in reversed(pw) for v in pair)
    return torch.nn.functional.pad(x, flat)

kernel_name = 'sequential_preLN__gqa_block_cached__mixer_token_mlp__decode'

def setup_inputs():

    _w = (lambda a: (lambda t: t.to(torch.bfloat16) if t.is_floating_point() else t)(torch.as_tensor(a)))
    def _A_setup():
        B, Sq, D = 8, 1, 448
        Hq, Hkv, Sk, Dh = 16, 8, 8192, 256
        assert Hq % Hkv == 0, (Hq, Hkv)
        return {
            "x": _w((lambda *s: torch.randn(*s, dtype=torch.bfloat16) * 0.05)(B, Sq, D)),
            "k_cache": _w((lambda *s: torch.randn(*s, dtype=torch.bfloat16) * 1.6)(B, Hkv, Sk, Dh)),
            "v_cache": _w((lambda *s: torch.randn(*s, dtype=torch.bfloat16) * 0.8)(B, Hkv, Sk, Dh)),
            "wq": _w((lambda *s: torch.randn(*s, dtype=torch.bfloat16) * 1.6)(D, Hq * Dh) * (D ** -0.5)),
            "wo": _w((lambda *s: torch.randn(*s, dtype=torch.bfloat16) * 0.8)(Hq * Dh, D) * ((Hq * Dh) ** -0.5)),
        }
    _A_in = _A_setup()

    def _B_setup():
        B, S, D, F = 8, 1, 448, 1792
        return {
            "x": _w((lambda *s: torch.randn(*s, dtype=torch.bfloat16) * 0.05)(B, S, D)),
            "w1": _w((lambda *s: torch.randn(*s, dtype=torch.bfloat16) * 0.4)(S, F) * (S ** -0.5)),
            "b1": _w((lambda *s: torch.randn(*s, dtype=torch.bfloat16) * 0.4)(F) * 0.02),
            "w2": _w((lambda *s: torch.randn(*s, dtype=torch.bfloat16) * 0.4)(F, S) * (F ** -0.5)),
            "b2": _w((lambda *s: torch.randn(*s, dtype=torch.bfloat16) * 0.4)(S) * 0.02),
        }
    _B_in = _B_setup()

    return {
        "A_x": _A_in["x"],
        "A_k_cache": _A_in["k_cache"],
        "A_v_cache": _A_in["v_cache"],
        "A_wq": _A_in["wq"],
        "A_wo": _A_in["wo"],
        "B_x": _B_in["x"],
        "B_w1": _B_in["w1"],
        "B_b1": _B_in["b1"],
        "B_w2": _B_in["w2"],
        "B_b2": _B_in["b2"],
    }

def reference(A_x, A_k_cache, A_v_cache, A_wq, A_wo, B_x, B_w1, B_b1, B_w2, B_b2):
    def _A_ref(x, k_cache, v_cache, wq, wo):
        B, Sq, D = x.shape
        Hq, Dh = 16, 256
        Hkv = k_cache.shape[1]
        rep = Hq // Hkv
        q = (lambda x, *d: x.permute(*d))((x @ wq).reshape(B, Sq, Hq, Dh), 0, 2, 1, 3)
        k = (lambda x, n, axis: torch.repeat_interleave(x, n, dim=axis))(k_cache, rep, axis=1)
        v = (lambda x, n, axis: torch.repeat_interleave(x, n, dim=axis))(v_cache, rep, axis=1)
        scores = torch.einsum("bhqd,bhkd->bhqk", q, k) * (Dh ** -0.5)
        p = _softmax(scores, axis=-1)
        o = torch.einsum("bhqk,bhkd->bhqd", p, v)
        return (lambda x, *d: x.permute(*d))(o, 0, 2, 1, 3).reshape(B, Sq, Hq * Dh) @ wo

    def _B_ref(x, w1, b1, w2, b2):
        B, S, D = x.shape
        h = _layernorm(x, axis=-1)
        u = torch.einsum("bsd,sf->bdf", h, w1) + b1
        u = _gelu(u)
        y = torch.einsum("bdf,fs->bds", u, w2) + b2
        return (lambda x, *d: x.permute(*d))(y, 0, 2, 1)

    x = A_x
    h = x + _A_ref(_layernorm(x, axis=-1), A_k_cache, A_v_cache, A_wq, A_wo)
    h2 = h + _B_ref(_layernorm(h, axis=-1), B_w1, B_b1, B_w2, B_b2)
    return h2
```

```cpp
#include <hip/hip_runtime.h>
#include <hip/hip_bf16.h>
#include <stdint.h>

namespace {
constexpr int NB = 8, DM = 448, HQ = 16, HKV = 8, SK = 8192, DH = 256, FF = 1792;
constexpr int NWG = 256, NT = 512, NWV = 8;
constexpr int NSPLIT = 4;
constexpr int TK = 16;
constexpr int NTILE = SK / TK / (NSPLIT * NWV);

typedef unsigned u32x4 __attribute__((ext_vector_type(4)));
typedef unsigned u32x2 __attribute__((ext_vector_type(2)));
typedef float f32x4 __attribute__((ext_vector_type(4)));
typedef float f32x2 __attribute__((ext_vector_type(2)));
typedef __bf16 bf16x2 __attribute__((ext_vector_type(2)));
typedef __bf16 bf16x8 __attribute__((ext_vector_type(8)));
typedef short s16x4 __attribute__((ext_vector_type(4)));
typedef unsigned short u16;
#define LAS __attribute__((address_space(3)))

__device__ __forceinline__ float rbf(float x) { return (float)(__bf16)x; }
__device__ __forceinline__ unsigned bfbits(float x) { return (unsigned)__builtin_bit_cast(u16, (__bf16)x); }
__device__ __forceinline__ float lo2f(unsigned u) { return __builtin_bit_cast(float, u << 16); }
__device__ __forceinline__ float hi2f(unsigned u) { return __builtin_bit_cast(float, u & 0xffff0000u); }
__device__ __forceinline__ float dot2(unsigned k, unsigned q, float acc) {
  return __builtin_amdgcn_fdot2_f32_bf16(__builtin_bit_cast(bf16x2, k), __builtin_bit_cast(bf16x2, q), acc, false);
}
template <int CTRL> __device__ __forceinline__ float dpp(float x) {
  return __builtin_bit_cast(float, __builtin_amdgcn_mov_dpp(__builtin_bit_cast(int, x), CTRL, 0xf, 0xf, true));
}
constexpr int XOR1 = 0xB1, XOR2 = 0x4E, XOR7 = 0x141, XOR8 = 0x128, ROR4 = 0x124;
__device__ __forceinline__ float x16_sum(float x) {
  auto s = __builtin_amdgcn_permlane16_swap(__float_as_uint(x), __float_as_uint(x), false, false);
  return __uint_as_float(s[0]) + __uint_as_float(s[1]);
}
__device__ __forceinline__ float x32_sum(float x) {
  auto t = __builtin_amdgcn_permlane32_swap(__float_as_uint(x), __float_as_uint(x), false, false);
  return __uint_as_float(t[0]) + __uint_as_float(t[1]);
}
__device__ __forceinline__ float x16_max(float x) {
  auto s = __builtin_amdgcn_permlane16_swap(__float_as_uint(x), __float_as_uint(x), false, false);
  return fmaxf(__uint_as_float(s[0]), __uint_as_float(s[1]));
}
__device__ __forceinline__ float x32_max(float x) {
  auto t = __builtin_amdgcn_permlane32_swap(__float_as_uint(x), __float_as_uint(x), false, false);
  return fmaxf(__uint_as_float(t[0]), __uint_as_float(t[1]));
}
__device__ __forceinline__ float wave_sum(float x) {
  x += dpp<XOR1>(x); x += dpp<XOR2>(x); x += dpp<XOR7>(x); x += dpp<XOR8>(x);
  return x32_sum(x16_sum(x));
}
__device__ __forceinline__ u32x4 ldnt(const void* p) { return __builtin_nontemporal_load(reinterpret_cast<const u32x4*>(p)); }

constexpr size_t WS_Q = 0;
constexpr size_t WS_P = 65536;
constexpr size_t WS_Y = WS_P + (size_t)NWG * 2 * 260 * 4;

__global__ void __launch_bounds__(NT, 2) k_qproj(const u16* __restrict__ x, const u16* __restrict__ wq, unsigned char* ws) {
  __shared__ __attribute__((aligned(16))) unsigned char smem[7168 + 14336 + 7 * 128 * 4];
  u16* xn = reinterpret_cast<u16*>(smem);
  u16* wsl = reinterpret_cast<u16*>(smem + 7168);
  float* red = reinterpret_cast<float*>(smem + 7168 + 14336);
  const int tid = threadIdx.x, lane = tid & 63, wave = __builtin_amdgcn_readfirstlane(tid >> 6);
  const int bid = blockIdx.x, kvh = bid & 7, j = bid >> 3;
  const int col0 = kvh * 512 + j * 16;
  u32x4 xv = {0, 0, 0, 0};
  if (lane < 56) xv = *reinterpret_cast<const u32x4*>(x + wave * DM + lane * 8);
  u32x4 w0 = {0, 0, 0, 0}, w1 = {0, 0, 0, 0};
  const int row0 = wave * 64 + (lane >> 1);
  if (wave < 7) {
    w0 = *reinterpret_cast<const u32x4*>(wq + (size_t)row0 * 4096 + col0 + (lane & 1) * 8);
    w1 = *reinterpret_cast<const u32x4*>(wq + (size_t)(row0 + 32) * 4096 + col0 + (lane & 1) * 8);
  }
  float xf[8];
#pragma unroll
  for (int k = 0; k < 4; ++k) { xf[2 * k] = lo2f(xv[k]); xf[2 * k + 1] = hi2f(xv[k]); }
  float s = 0.f;
#pragma unroll
  for (int k = 0; k < 8; ++k) s += xf[k];
  const float mean = wave_sum(s) * (1.f / DM);
  float vs = 0.f;
#pragma unroll
  for (int k = 0; k < 8; ++k) { const float d = xf[k] - mean; vs += d * d; }
  if (lane >= 56) vs = 0.f;
  const float var = wave_sum(vs) * (1.f / DM);
  const float mb = rbf(mean), den = rbf(sqrtf(rbf(rbf(var) + 1e-5f)));
  u32x4 xo;
#pragma unroll
  for (int k = 0; k < 4; ++k) xo[k] = bfbits(rbf(xf[2 * k] - mb) / den) | (bfbits(rbf(xf[2 * k + 1] - mb) / den) << 16);
  if (lane < 56) *reinterpret_cast<u32x4*>(xn + wave * DM + lane * 8) = xo;
  if (wave < 7) {
    *reinterpret_cast<u32x4*>(wsl + row0 * 16 + (lane & 1) * 8) = w0;
    *reinterpret_cast<u32x4*>(wsl + (row0 + 32) * 16 + (lane & 1) * 8) = w1;
  }
  __syncthreads();
  if (wave < 7) {
    f32x4 acc = {0.f, 0.f, 0.f, 0.f};
    const int G = lane >> 4, i = lane & 15;
#pragma unroll
    for (int st = 0; st < 2; ++st) {
      const int k0 = wave * 64 + st * 32;
      const bf16x8 a = *reinterpret_cast<const bf16x8*>(xn + (i & 7) * DM + k0 + 8 * G);
      const int r = k0 + 8 * G + (i >> 2);
      LAS s16x4* p0 = (LAS s16x4*)(wsl + r * 16 + (i & 3) * 4);
      LAS s16x4* p1 = (LAS s16x4*)(wsl + (r + 4) * 16 + (i & 3) * 4);
      const s16x4 b0 = __builtin_amdgcn_ds_read_tr16_b64_v4i16(p0);
      const s16x4 b1 = __builtin_amdgcn_ds_read_tr16_b64_v4i16(p1);
      typedef short s16x8 __attribute__((ext_vector_type(8)));
      const s16x8 bb = {b0[0], b0[1], b0[2], b0[3], b1[0], b1[1], b1[2], b1[3]};
      acc = __builtin_amdgcn_mfma_f32_16x16x32_bf16(a, __builtin_bit_cast(bf16x8, bb), acc, 0, 0, 0);
    }
    if (lane < 32) {
#pragma unroll
      for (int r = 0; r < 4; ++r) red[wave * 128 + (4 * G + r) * 16 + i] = acc[r];
    }
  }
  __syncthreads();
  if (tid < 128) {
    float t = 0.f;
#pragma unroll
    for (int w = 0; w < 7; ++w) t += red[w * 128 + tid];
    reinterpret_cast<u16*>(ws + WS_Q)[(tid >> 4) * 4096 + col0 + (tid & 15)] = (u16)bfbits(t);
  }
}

__global__ void __launch_bounds__(NT, 2) k_attn(const u16* __restrict__ kc, const u16* __restrict__ vc, unsigned char* ws) {
  __shared__ __attribute__((aligned(16))) float smem[NWV * 32 + NWV * 2 * 260];
  const int tid = threadIdx.x, lane = tid & 63, wave = __builtin_amdgcn_readfirstlane(tid >> 6);
  const int bid = blockIdx.x, kvh = bid & 7, b = (bid >> 3) & 7, s = bid >> 6;
  const int r = lane >> 4, c = lane & 15, r2 = lane >> 5, c2 = lane & 31;
  const size_t pair_off = (size_t)(b * HKV + kvh) * SK * DH * 2;
  const size_t tile0 = (size_t)(s * NWV + wave) * (TK * DH * 2);
  const unsigned char* kp = reinterpret_cast<const unsigned char*>(kc) + pair_off + tile0 + r * 512 + c * 16;
  const unsigned char* vp = reinterpret_cast<const unsigned char*>(vc) + pair_off + tile0 + lane * 16;
  constexpr size_t TSTRIDE = (size_t)NSPLIT * NWV * TK * DH * 2;
  u32x4 q[2][2];
  {
    const u16* qg = reinterpret_cast<const u16*>(ws + WS_Q) + b * 4096 + kvh * 512;
#pragma unroll
    for (int h = 0; h < 2; ++h)
#pragma unroll
      for (int j = 0; j < 2; ++j) q[h][j] = *reinterpret_cast<const u32x4*>(qg + h * 256 + (c + 16 * j) * 8);
  }
  u32x4 K[8], V[8];
#pragma unroll
  for (int i = 0; i < 8; ++i) K[i] = ldnt(kp + (i >> 1) * 2048 + (i & 1) * 256);
#pragma unroll
  for (int i = 0; i < 8; ++i) V[i] = ldnt(vp + i * 1024);
  float m = -INFINITY, l = 0.f;
  float acc[2][8];
#pragma unroll
  for (int h = 0; h < 2; ++h)
#pragma unroll
    for (int k = 0; k < 8; ++k) acc[h][k] = 0.f;
  float* pl = smem + wave * 32;
  const int pw = (r & 1) * 16 + ((c >> 2) * 2 + (r >> 1)) * 2 + ((c >> 1) & 1);
  constexpr float C2 = 0.0625f * 1.4426950408889634f;

  auto tile = [&](auto PF) {
    constexpr bool pf = decltype(PF)::value;
    float x[8];
#pragma unroll
    for (int g = 0; g < 4; ++g) {
#pragma unroll
      for (int h = 0; h < 2; ++h) {
        float d = 0.f;
#pragma unroll
        for (int j = 0; j < 2; ++j)
#pragma unroll
          for (int k = 0; k < 4; ++k) d = dot2(K[2 * g + j][k], q[h][j][k], d);
        x[2 * g + h] = d;
      }
      if constexpr (pf) {
        K[2 * g] = ldnt(kp + g * 2048);
        K[2 * g + 1] = ldnt(kp + g * 2048 + 256);
      }
    }
#define TR_STEP(HALF, CTRL, BIT) _Pragma("unroll") for (int i = 0; i < (HALF); ++i) { \
      const float lo_ = x[i], hi_ = x[i + (HALF)]; const float t1_ = lo_ + dpp<CTRL>(lo_), t2_ = hi_ + dpp<CTRL>(hi_); \
      x[i] = (c & (BIT)) ? t2_ : t1_; }
    TR_STEP(4, XOR8, 8) TR_STEP(2, XOR7, 4) TR_STEP(1, XOR2, 2)
#undef TR_STEP
    float sc = x[0] + dpp<XOR1>(x[0]);
    sc = rbf(sc) * C2;
    if (__any(sc > m)) {
      float pm = sc;
      pm = fmaxf(pm, dpp<ROR4>(pm)); pm = fmaxf(pm, dpp<XOR8>(pm));
      pm = x32_max(x16_max(pm));
      const float mn = fmaxf(m, pm);
      const float alpha = __builtin_amdgcn_exp2f(m - mn);
      m = mn; l *= alpha;
      const float a0 = dpp<0x00>(alpha), a1 = dpp<0xAA>(alpha);
#pragma unroll
      for (int k = 0; k < 8; ++k) { acc[0][k] *= a0; acc[1][k] *= a1; }
    }
    const float p = __builtin_amdgcn_exp2f(sc - m);
    l += p;
    if ((c & 1) == 0) pl[pw] = p;
    asm volatile("s_waitcnt lgkmcnt(0)" ::: "memory");
#pragma unroll
    for (int i2 = 0; i2 < 4; ++i2) {
      const f32x4 pp = *reinterpret_cast<const f32x4*>(pl + r2 * 16 + i2 * 4);
#pragma unroll
      for (int e = 0; e < 2; ++e) {
        const int i = 2 * i2 + e;
        const float p0 = pp[2 * e], p1 = pp[2 * e + 1];
#pragma unroll
        for (int k = 0; k < 4; ++k) {
          const float v0 = lo2f(V[i][k]), v1 = hi2f(V[i][k]);
          acc[0][2 * k] = fmaf(p0, v0, acc[0][2 * k]); acc[0][2 * k + 1] = fmaf(p0, v1, acc[0][2 * k + 1]);
          acc[1][2 * k] = fmaf(p1, v0, acc[1][2 * k]); acc[1][2 * k + 1] = fmaf(p1, v1, acc[1][2 * k + 1]);
        }
        if constexpr (pf) V[i] = ldnt(vp + i * 1024);
      }
    }
  };
  for (int t = 0; t < NTILE - 1; ++t) {
    kp += TSTRIDE; vp += TSTRIDE;
    tile(std::integral_constant<bool, true>{});
  }
  tile(std::integral_constant<bool, false>{});

#pragma unroll
  for (int h = 0; h < 2; ++h)
#pragma unroll
    for (int k = 0; k < 8; ++k) acc[h][k] = x32_sum(acc[h][k]);
  l += dpp<ROR4>(l); l += dpp<XOR8>(l); l = x32_sum(x16_sum(l));
  float* mrg = smem + NWV * 32;
  if (lane < 32) {
#pragma unroll
    for (int h = 0; h < 2; ++h) {
      float* rec = mrg + (wave * 2 + h) * 260;
      *reinterpret_cast<f32x4*>(rec + 4 + c2 * 8) = f32x4{acc[h][0], acc[h][1], acc[h][2], acc[h][3]};
      *reinterpret_cast<f32x4*>(rec + 8 + c2 * 8) = f32x4{acc[h][4], acc[h][5], acc[h][6], acc[h][7]};
    }
    if (lane == 0 || lane == 2) { float* rec = mrg + (wave * 2 + (lane >> 1)) * 260; rec[0] = m; rec[1] = l; }
  }
  __syncthreads();
  {
    const int h = tid >> 8, d = tid & 255;
    float M = -INFINITY;
#pragma unroll
    for (int w = 0; w < NWV; ++w) M = fmaxf(M, mrg[(w * 2 + h) * 260]);
    float L = 0.f, o = 0.f;
#pragma unroll
    for (int w = 0; w < NWV; ++w) {
      const float* rec = mrg + (w * 2 + h) * 260;
      const float wt = __builtin_amdgcn_exp2f(rec[0] - M);
      L = fmaf(wt, rec[1], L); o = fmaf(wt, rec[4 + d], o);
    }
    float* pg = reinterpret_cast<float*>(ws + WS_P) + ((size_t)bid * 2 + h) * 260;
    pg[4 + d] = o;
    if (d == 0) { pg[0] = M; pg[1] = L; }
  }
}

__global__ void __launch_bounds__(NT, 2) k_comb(const u16* __restrict__ wo, unsigned char* ws) {
  __shared__ __attribute__((aligned(16))) unsigned char smem[1024 + NWV * 128 * 4];
  u16* ob = reinterpret_cast<u16*>(smem);
  float* red = reinterpret_cast<float*>(smem + 1024);
  const int tid = threadIdx.x, lane = tid & 63, wave = __builtin_amdgcn_readfirstlane(tid >> 6);
  const int bid = blockIdx.x, kvh = bid & 7, b = (bid >> 3) & 7, s = bid >> 6;
  const int rg = lane >> 4, c = lane & 15;
  u32x4 W[8][2];
  const int cc = c < 14 ? c : 13;
  const u16* wp = wo + (size_t)(kvh * 512 + 8 * wave + 2 * rg) * DM + s * 112 + cc * 8;
#pragma unroll
  for (int mm = 0; mm < 8; ++mm) {
    W[mm][0] = *reinterpret_cast<const u32x4*>(wp + (size_t)mm * 64 * DM);
    W[mm][1] = *reinterpret_cast<const u32x4*>(wp + (size_t)mm * 64 * DM + DM);
  }
  {
    const int h = tid >> 8, d = tid & 255;
    const float* pg = reinterpret_cast<const float*>(ws + WS_P);
    float ms[4], ls[4], as[4];
#pragma unroll
    for (int t = 0; t < 4; ++t) {
      const float* rec = pg + ((size_t)(t * 64 + (bid & 63)) * 2 + h) * 260;
      ms[t] = rec[0]; ls[t] = rec[1]; as[t] = rec[4 + d];
    }
    const float M = fmaxf(fmaxf(ms[0], ms[1]), fmaxf(ms[2], ms[3]));
    float L = 0.f, o = 0.f;
#pragma unroll
    for (int t = 0; t < 4; ++t) { const float wt = __builtin_amdgcn_exp2f(ms[t] - M); L = fmaf(wt, ls[t], L); o = fmaf(wt, as[t], o); }
    ob[tid] = (u16)bfbits(o / L);
  }
  __syncthreads();
  float acc[8];
#pragma unroll
  for (int k = 0; k < 8; ++k) acc[k] = 0.f;
#pragma unroll
  for (int mm = 0; mm < 8; ++mm) {
    const unsigned op = *reinterpret_cast<const unsigned*>(ob + 64 * mm + 8 * wave + 2 * rg);
#pragma unroll
    for (int k = 0; k < 4; ++k) {
      const unsigned lo = __builtin_amdgcn_perm(W[mm][1][k], W[mm][0][k], 0x05040100u);
      const unsigned hi = __builtin_amdgcn_perm(W[mm][1][k], W[mm][0][k], 0x07060302u);
      acc[2 * k] = dot2(lo, op, acc[2 * k]); acc[2 * k + 1] = dot2(hi, op, acc[2 * k + 1]);
    }
  }
#pragma unroll
  for (int k = 0; k < 8; ++k) acc[k] = x32_sum(x16_sum(acc[k]));
  if (lane < 14) {
    *reinterpret_cast<f32x4*>(red + wave * 128 + lane * 8) = f32x4{acc[0], acc[1], acc[2], acc[3]};
    *reinterpret_cast<f32x4*>(red + wave * 128 + lane * 8 + 4) = f32x4{acc[4], acc[5], acc[6], acc[7]};
  }
  __syncthreads();
  if (tid < 112) {
    float t = 0.f;
#pragma unroll
    for (int w = 0; w < NWV; ++w) t += red[w * 128 + tid];
    reinterpret_cast<float*>(ws + WS_Y)[(b * 8 + kvh) * DM + s * 112 + tid] = t;
  }
}

__global__ void __launch_bounds__(NT, 2) k_final(const u16* __restrict__ x, const u16* __restrict__ w1, const u16* __restrict__ b1,
                                                 const u16* __restrict__ w2, const u16* __restrict__ b2, u16* __restrict__ out, unsigned char* ws) {
  __shared__ __attribute__((aligned(16))) float smem[448 + 64];
  float* hs = smem; float* red = smem + 448;
  const int tid = threadIdx.x, lane = tid & 63, wave = __builtin_amdgcn_readfirstlane(tid >> 6);
  const int bid = blockIdx.x, b = (bid >> 3) & 7, j = (bid & 7) * 4 + (bid >> 6);
  constexpr float KC = 0.7978845608028654f, L2E = 1.4426950408889634f;
  constexpr float K1 = -2.f * KC * L2E, K2 = -2.f * KC * 0.044715f * L2E;
  float c0[7], c1[7], c2[7], c3[7], a0[7], a1[7];
#pragma unroll
  for (int i = 0; i < 7; ++i) {
    const int f = (tid & 255) + 256 * i;
    const float w = lo2f(w1[f]), bb = lo2f(b1[f]), v = lo2f(w2[f]);
    c3[i] = K2 * w * w * w; c2[i] = 3.f * K2 * w * w * bb; c1[i] = K1 * w + 3.f * K2 * w * bb * bb; c0[i] = K1 * bb + K2 * bb * bb * bb;
    a1[i] = w * v; a0[i] = bb * v;
  }
  if (tid < DM) {
    const float* yg = reinterpret_cast<const float*>(ws + WS_Y) + b * 8 * DM + tid;
    float t = 0.f;
#pragma unroll
    for (int k = 0; k < 8; ++k) t += yg[k * DM];
    hs[tid] = rbf(lo2f(x[b * DM + tid]) + rbf(t));
  }
  __syncthreads();
  float v[7];
#pragma unroll
  for (int i = 0; i < 7; ++i) v[i] = hs[lane + 64 * i];
#pragma unroll
  for (int rep = 0; rep < 2; ++rep) {
    float s = 0.f;
#pragma unroll
    for (int i = 0; i < 7; ++i) s += v[i];
    const float mean = wave_sum(s) * (1.f / DM);
    float vs = 0.f;
#pragma unroll
    for (int i = 0; i < 7; ++i) { const float d = v[i] - mean; vs += d * d; }
    const float var = wave_sum(vs) * (1.f / DM);
    const float mb = rbf(mean), den = rbf(sqrtf(rbf(rbf(var) + 1e-5f)));
#pragma unroll
    for (int i = 0; i < 7; ++i) v[i] = rbf(rbf(v[i] - mb) / den);
  }
  const int par = wave >> 2;
  float acc[7];
#pragma unroll
  for (int mo = 0; mo < 7; ++mo) {
    const int d = 14 * j + 2 * mo + par;
    float hv = 0.f;
#pragma unroll
    for (int i = 0; i < 7; ++i) if ((d >> 6) == i) hv = v[i];
    const float h1 = __builtin_bit_cast(float, __builtin_amdgcn_readlane(__builtin_bit_cast(int, hv), d & 63));
    const float h2 = h1 * h1, h3 = h2 * h1;
    float a = 0.f;
#pragma unroll
    for (int i = 0; i < 7; ++i) {
      const float z = fmaf(c3[i], h3, fmaf(c2[i], h2, fmaf(c1[i], h1, c0[i])));
      const float e = __builtin_amdgcn_exp2f(z);
      const float rr = __builtin_amdgcn_rcpf(1.f + e);
      a = fmaf(fmaf(a1[i], h1, a0[i]), rr, a);
    }
    acc[mo] = a;
  }
#pragma unroll
  for (int mo = 0; mo < 7; ++mo) acc[mo] = wave_sum(acc[mo]);
  if (lane == 0) {
#pragma unroll
    for (int mo = 0; mo < 7; ++mo) red[wave * 8 + mo] = acc[mo];
  }
  __syncthreads();
  if (tid < 14) {
    const int mo = tid >> 1, pr = tid & 1;
    const float y = red[(pr * 4 + 0) * 8 + mo] + red[(pr * 4 + 1) * 8 + mo] + red[(pr * 4 + 2) * 8 + mo] + red[(pr * 4 + 3) * 8 + mo];
    const int d = 14 * j + tid;
    const float y2 = rbf(rbf(y) + lo2f(b2[0]));
    out[b * DM + d] = (u16)bfbits(hs[d] + y2);
  }
}
}

extern "C" void kernel_launch(void* const* d_in, const int* in_sizes, int n_in, void* d_out, int out_size, void* d_ws, size_t ws_size,
                              hipStream_t stream) {
  const u16* x = (const u16*)d_in[0];
  const u16* kc = (const u16*)d_in[1];
  const u16* vc = (const u16*)d_in[2];
  const u16* wq = (const u16*)d_in[3];
  const u16* wo = (const u16*)d_in[4];
  const u16* w1 = (const u16*)d_in[6];
  const u16* b1 = (const u16*)d_in[7];
  const u16* w2 = (const u16*)d_in[8];
  const u16* b2 = (const u16*)d_in[9];
  unsigned char* ws = (unsigned char*)d_ws;
  k_qproj<<<NWG, NT, 0, stream>>>(x, wq, ws);
  k_attn<<<NWG, NT, 0, stream>>>(kc, vc, ws);
  k_comb<<<NWG, NT, 0, stream>>>(wo, ws);
  k_final<<<NWG, NT, 0, stream>>>(x, w1, b1, w2, b2, (u16*)d_out, ws);
}
```

```cpp
#include <hip/hip_runtime.h>
#include <hip/hip_bf16.h>
#include <stdint.h>
#include <type_traits>

#define STAMPS 0

namespace {
constexpr int NB = 8, DM = 448, HQ = 16, HKV = 8, SK = 8192, DH = 256, FF = 1792;
constexpr int NWG = 256, NT = 512, NWV = 8;
constexpr int NSPLIT = 4;
constexpr int TK = 16;
constexpr int NTILE = SK / TK / (NSPLIT * NWV);

typedef unsigned u32x4 __attribute__((ext_vector_type(4)));
typedef float f32x4 __attribute__((ext_vector_type(4)));
typedef float f32x2 __attribute__((ext_vector_type(2)));
typedef __bf16 bf16x2 __attribute__((ext_vector_type(2)));
typedef __bf16 bf16x8 __attribute__((ext_vector_type(8)));
typedef short s16x4 __attribute__((ext_vector_type(4)));
typedef short s16x8 __attribute__((ext_vector_type(8)));
typedef unsigned short u16;
typedef unsigned long long u64;
#define LAS __attribute__((address_space(3)))
#define GAS __attribute__((address_space(1)))
typedef GAS u64 gu64;
#define RLX_AGENT __ATOMIC_RELAXED, __HIP_MEMORY_SCOPE_AGENT

__device__ __forceinline__ float rbf(float x) { return (float)(__bf16)x; }
__device__ __forceinline__ unsigned bfbits(float x) { return (unsigned)__builtin_bit_cast(u16, (__bf16)x); }
__device__ __forceinline__ float lo2f(unsigned u) { return __builtin_bit_cast(float, u << 16); }
__device__ __forceinline__ float hi2f(unsigned u) { return __builtin_bit_cast(float, u & 0xffff0000u); }
__device__ __forceinline__ float dot2(unsigned k, unsigned q, float acc) {
  return __builtin_amdgcn_fdot2_f32_bf16(__builtin_bit_cast(bf16x2, k), __builtin_bit_cast(bf16x2, q), acc, false);
}
template <int CTRL> __device__ __forceinline__ float dpp(float x) {
  return __builtin_bit_cast(float, __builtin_amdgcn_mov_dpp(__builtin_bit_cast(int, x), CTRL, 0xf, 0xf, true));
}
constexpr int XOR1 = 0xB1, XOR2 = 0x4E, XOR7 = 0x141, XOR8 = 0x128, ROR4 = 0x124;
__device__ __forceinline__ float x16_sum(float x) {
  auto s = __builtin_amdgcn_permlane16_swap(__float_as_uint(x), __float_as_uint(x), false, false);
  return __uint_as_float(s[0]) + __uint_as_float(s[1]);
}
__device__ __forceinline__ float x32_sum(float x) {
  auto t = __builtin_amdgcn_permlane32_swap(__float_as_uint(x), __float_as_uint(x), false, false);
  return __uint_as_float(t[0]) + __uint_as_float(t[1]);
}
__device__ __forceinline__ float x16_max(float x) {
  auto s = __builtin_amdgcn_permlane16_swap(__float_as_uint(x), __float_as_uint(x), false, false);
  return fmaxf(__uint_as_float(s[0]), __uint_as_float(s[1]));
}
__device__ __forceinline__ float x32_max(float x) {
  auto t = __builtin_amdgcn_permlane32_swap(__float_as_uint(x), __float_as_uint(x), false, false);
  return fmaxf(__uint_as_float(t[0]), __uint_as_float(t[1]));
}
__device__ __forceinline__ float wave_sum(float x) {
  x += dpp<XOR1>(x); x += dpp<XOR2>(x); x += dpp<XOR7>(x); x += dpp<XOR8>(x);
  return x32_sum(x16_sum(x));
}
__device__ __forceinline__ u32x4 ldnt(const void* p) { return __builtin_nontemporal_load(reinterpret_cast<const u32x4*>(p)); }
__device__ __forceinline__ u64 gload(const unsigned char* p) { return __hip_atomic_load((gu64*)p, RLX_AGENT); }
__device__ __forceinline__ void gstore(unsigned char* p, u64 v) { __hip_atomic_store((gu64*)p, v, RLX_AGENT); }
#define SB() __builtin_amdgcn_sched_barrier(0)

constexpr size_t WS_EPOCH = 0;
constexpr size_t WS_READY = 4096;
constexpr size_t WS_Q = 8192;
constexpr int PREC = 544;
constexpr size_t WS_P = WS_Q + (size_t)64 * 512 * 8;
constexpr size_t WS_Y = WS_P + (size_t)NWG * PREC * 4;
constexpr size_t WS_END = WS_Y + (size_t)NB * DM * 8 * 4;
constexpr size_t WS_STAMP = (size_t)16 << 20;
constexpr unsigned SPIN_CAP = 1u << 18;
constexpr unsigned EMPTY = 0xffffffffu;
constexpr int NTW = SK / TK / NSPLIT;

constexpr int L_XN = 0;
constexpr int L_WSL = 7168;
constexpr int L_RED = L_WSL + 14336;
constexpr int L_QB = L_RED + 3584;
constexpr int L_PL = L_QB + 1024;
constexpr int L_MRG = L_PL + 1024;
constexpr int L_OB = L_MRG + NWV * 2 * 260 * 4;
constexpr int L_RED2 = L_OB + 1024;
constexpr int L_HS = L_RED2 + 4096;
constexpr int L_RED3 = L_HS + 1792;
constexpr int L_MW = L_RED3 + 256;
constexpr int L_XR = L_MW + 3 * FF * 2;
constexpr int L_EP = L_XR + 896;
constexpr int L_END = L_EP + 16;
#define POLL_DELAY 8
#define SPIN_SLEEP 2
#define RDY_AT 80

struct Params {
  const u16* x; const u16* kc; const u16* vc; const u16* wq; const u16* wo;
  const u16* w1; const u16* b1; const u16* w2; const u16* b2;
  u16* out; unsigned char* ws;
};

#define STAMP(i) do {} while (0)
__device__ __forceinline__ unsigned pubbits(float v) { return min(__float_as_uint(v), 0xfffffffeu); }
__device__ __forceinline__ bool full4(u32x4 v) { return v[0] != EMPTY && v[1] != EMPTY && v[2] != EMPTY && v[3] != EMPTY; }
template <int N> using IC = std::integral_constant<int, N>;

__global__ void __launch_bounds__(NT, 2) k_fused(Params prm) {
  __shared__ __attribute__((aligned(16))) unsigned char smem[L_END];
  unsigned char* const ws = prm.ws;
  const int tid = threadIdx.x, lane = tid & 63, wave = __builtin_amdgcn_readfirstlane(tid >> 6);
  const int bid = blockIdx.x, kvh = bid & 7, b = (bid >> 3) & 7, s = bid >> 6, jq = bid >> 3;
  const int r = lane >> 4, c = lane & 15, r2 = lane >> 5, c2 = lane & 31;
  STAMP(0);
  const int col0 = kvh * 512 + jq * 16;
  u32x4 xv = *reinterpret_cast<const u32x4*>(prm.x + wave * DM + (lane < 56 ? lane : 55) * 8);
  const int row0 = (wave < 7 ? wave : 6) * 64 + (lane >> 1);
  u32x4 w0 = *reinterpret_cast<const u32x4*>(prm.wq + (size_t)row0 * 4096 + col0 + (lane & 1) * 8);
  u32x4 w1v = *reinterpret_cast<const u32x4*>(prm.wq + (size_t)(row0 + 32) * 4096 + col0 + (lane & 1) * 8);
  const u64 ep0 = gload(ws + (wave == 7 ? WS_EPOCH : WS_READY + (size_t)bid * 8));
  const __amdgpu_buffer_rsrc_t wsr = __builtin_amdgcn_make_buffer_rsrc(ws, 0, (int)WS_END, 0x00020000);
  constexpr int SC1 = 16;
  const unsigned prec = (unsigned)WS_P + (unsigned)bid * PREC * 4;
  const unsigned yout = (unsigned)WS_Y + (unsigned)((b * DM + s * 112) * 8 + kvh) * 4;
  SB();
  __builtin_amdgcn_s_barrier();
  SB();
  const size_t pair_off = (size_t)(b * HKV + kvh) * SK * DH * 2 + (size_t)s * NWV * (TK * DH * 2);
  const unsigned char* const kbase = reinterpret_cast<const unsigned char*>(prm.kc) + pair_off;
  const unsigned char* const vbase = reinterpret_cast<const unsigned char*>(prm.vc) + pair_off;
  auto tile_off = [](int T) { return (size_t)(unsigned)(((T >> 3) * 32 + (T & 7)) * (TK * DH * 2)); };
  const unsigned koff = r * 512 + c * 16, voff = lane * 16;
  u32x4 K[8], V[8], KB[8], VB[8];
  {
    const unsigned char* kp = kbase + tile_off(wave);
#pragma unroll
    for (int i = 0; i < 8; ++i) K[i] = ldnt(kp + (i >> 1) * 2048 + (i & 1) * 256 + koff);
  }
  SB();
  u64 epoch;
  {
    u16* xn = reinterpret_cast<u16*>(smem + L_XN);
    u16* wsl = reinterpret_cast<u16*>(smem + L_WSL);
    float* red = reinterpret_cast<float*>(smem + L_RED);
    float xf[8];
    STAMP(1);
#pragma unroll
    for (int k = 0; k < 4; ++k) { xf[2 * k] = lo2f(xv[k]); xf[2 * k + 1] = hi2f(xv[k]); }
    float sm = 0.f;
#pragma unroll
    for (int k = 0; k < 8; ++k) sm += xf[k];
    if (lane >= 56) sm = 0.f;
    const float mean = wave_sum(sm) * (1.f / DM);
    float vs = 0.f;
#pragma unroll
    for (int k = 0; k < 8; ++k) { const float d = xf[k] - mean; vs += d * d; }
    if (lane >= 56) vs = 0.f;
    const float var = wave_sum(vs) * (1.f / DM);
    const float mb = rbf(mean), rden = 1.f / rbf(sqrtf(rbf(rbf(var) + 1e-5f)));
    u32x4 xo;
#pragma unroll
    for (int k = 0; k < 4; ++k) xo[k] = bfbits(rbf(xf[2 * k] - mb) * rden) | (bfbits(rbf(xf[2 * k + 1] - mb) * rden) << 16);
    if (lane < 56) *reinterpret_cast<u32x4*>(xn + wave * DM + lane * 8) = xo;
    if (wave == b && lane < 56) *reinterpret_cast<u32x4*>(smem + L_XR + lane * 16) = xv;
    if (wave == 7 && lane == 0) { *reinterpret_cast<u64*>(smem + L_EP) = ep0; *reinterpret_cast<unsigned*>(smem + L_EP + 12) = 2 * NWV; }
    if (wave < 7) {
      *reinterpret_cast<u32x4*>(wsl + row0 * 16 + (lane & 1) * 8) = w0;
      *reinterpret_cast<u32x4*>(wsl + (row0 + 32) * 16 + (lane & 1) * 8) = w1v;
    }
    STAMP(2);
    __syncthreads();
    epoch = *reinterpret_cast<const u64*>(smem + L_EP) + 1ull;
    if (wave < 7) {
      f32x4 acc = {0.f, 0.f, 0.f, 0.f};
#pragma unroll
      for (int st = 0; st < 2; ++st) {
        const int k0 = wave * 64 + st * 32;
        const bf16x8 a = *reinterpret_cast<const bf16x8*>(xn + (c & 7) * DM + k0 + 8 * r);
        const int rr = k0 + 8 * r + (c >> 2);
        LAS s16x4* p0 = (LAS s16x4*)(wsl + rr * 16 + (c & 3) * 4);
        LAS s16x4* p1 = (LAS s16x4*)(wsl + (rr + 4) * 16 + (c & 3) * 4);
        const s16x4 b0 = __builtin_amdgcn_ds_read_tr16_b64_v4i16(p0);
        const s16x4 b1 = __builtin_amdgcn_ds_read_tr16_b64_v4i16(p1);
        const s16x8 bb = {b0[0], b0[1], b0[2], b0[3], b1[0], b1[1], b1[2], b1[3]};
        acc = __builtin_amdgcn_mfma_f32_16x16x32_bf16(a, __builtin_bit_cast(bf16x8, bb), acc, 0, 0, 0);
      }
      if (lane < 32) {
#pragma unroll
        for (int e = 0; e < 4; ++e) red[wave * 128 + (4 * r + e) * 16 + c] = acc[e];
      }
    }
    __syncthreads();
    STAMP(3);
    if (tid < 128) {
      float t = 0.f;
#pragma unroll
      for (int w = 0; w < 7; ++w) t += red[w * 128 + tid];
      gstore(ws + WS_Q + ((size_t)(kvh * 8 + (tid >> 4)) * 512 + jq * 16 + (tid & 15)) * 8, (epoch << 16) | (u64)bfbits(t));
    }
  }
  STAMP(4);
  {
    const unsigned char* qg = ws + WS_Q + ((size_t)(kvh * 8 + b) * 512 + tid) * 8;
    const u64 want = epoch & 0xffffffffffffull;
    SB();
    __builtin_amdgcn_s_sleep(POLL_DELAY);
    SB();
    u64 g = gload(qg);
    u32x4 mw[3];
    const int mi = tid < 224 ? tid : 223;
    mw[0] = *reinterpret_cast<const u32x4*>(prm.w1 + mi * 8);
    mw[1] = *reinterpret_cast<const u32x4*>(prm.b1 + mi * 8);
    mw[2] = *reinterpret_cast<const u32x4*>(prm.w2 + mi * 8);
    const unsigned b2r = prm.b2[0];
    SB();
    {
      const unsigned char* vp = vbase + tile_off(wave);
#pragma unroll
      for (int i = 0; i < 8; ++i) V[i] = ldnt(vp + i * 1024 + voff);
    }
    SB();
    if (wave == 7) {
      const u32x4 e4 = {EMPTY, EMPTY, EMPTY, EMPTY};
      __builtin_amdgcn_raw_buffer_store_b128(e4, wsr, prec + lane * 16, 0, SC1);
      __builtin_amdgcn_raw_buffer_store_b128(e4, wsr, prec + 1024 + lane * 16, 0, SC1);
      if (lane == 0) __builtin_amdgcn_raw_buffer_store_b128(e4, wsr, prec + 2048, 0, SC1);
      __builtin_amdgcn_raw_buffer_store_b32(EMPTY, wsr, yout + lane * 32, 0, SC1);
      if (lane < 48) __builtin_amdgcn_raw_buffer_store_b32(EMPTY, wsr, yout + (64 + lane) * 32, 0, SC1);
    }
  SB();
    bool ok = __all((g >> 16) == want);
    SB();
    const u64 g2 = gload(qg);
    {
      const unsigned char* kp = kbase + tile_off(NWV + wave);
#pragma unroll
      for (int i = 0; i < 8; ++i) KB[i] = ldnt(kp + (i >> 1) * 2048 + (i & 1) * 256 + koff);
    }
    SB();
    if (!ok) { g = g2; ok = __all((g >> 16) == want); }
    SB();
    const u64 g3 = gload(qg);
    {
      const unsigned char* vp = vbase + tile_off(NWV + wave);
#pragma unroll
      for (int i = 0; i < 8; ++i) VB[i] = ldnt(vp + i * 1024 + voff);
    }
    SB();
    if (!ok) {
      g = g3;
      if (!__all((g >> 16) == want)) {
        unsigned spins = 0;
        do {
          __builtin_amdgcn_s_sleep(1);
          g = gload(qg);
        } while (!__all((g >> 16) == want) && ++spins < SPIN_CAP);
      }
    }
    reinterpret_cast<u16*>(smem + L_QB)[tid] = (u16)g;
    if (tid == 0) *reinterpret_cast<unsigned*>(smem + L_EP + 8) = b2r;
    if (tid < 224) {
#pragma unroll
      for (int i = 0; i < 3; ++i) *reinterpret_cast<u32x4*>(smem + L_MW + i * FF * 2 + tid * 16) = mw[i];
    }
  }
  __syncthreads();
  u32x4 q[2][2];
#pragma unroll
  for (int h = 0; h < 2; ++h)
#pragma unroll
    for (int j = 0; j < 2; ++j) q[h][j] = *reinterpret_cast<const u32x4*>(smem + L_QB + (h * 256 + (c + 16 * j) * 8) * 2);
  STAMP(5);
  float m = -INFINITY, l = 0.f;
  f32x2 acc[2][4];
#pragma unroll
  for (int h = 0; h < 2; ++h)
#pragma unroll
    for (int k = 0; k < 4; ++k) acc[h][k] = f32x2{0.f, 0.f};
  float* pl = reinterpret_cast<float*>(smem + L_PL) + wave * 32;
  const int pw = (r & 1) * 16 + ((c >> 2) * 2 + (r >> 1)) * 2 + ((c >> 1) & 1);
  constexpr float C2 = 0.0625f * 1.4426950408889634f;
  const u16* wp = prm.wo + (size_t)(kvh * 512 + 8 * wave + 2 * r) * DM + s * 112 + (c < 14 ? c : 13) * 8;

  auto tile = [&](u32x4 (&Kr)[8], u32x4 (&Vr)[8], auto MODE, int T) {
    constexpr int mode = decltype(MODE)::value;
    const unsigned char* kp = kbase + tile_off(T);
    const unsigned char* vp = vbase + tile_off(T);
    float x[8];
#pragma unroll
    for (int g = 0; g < 4; ++g) {
#pragma unroll
      for (int h = 0; h < 2; ++h) {
        float d = 0.f;
#pragma unroll
        for (int j = 0; j < 2; ++j)
#pragma unroll
          for (int k = 0; k < 4; ++k) d = dot2(Kr[2 * g + j][k], q[h][j][k], d);
        x[2 * g + h] = d;
      }
      if constexpr (mode == 0) {
        Kr[2 * g] = ldnt(kp + g * 2048 + koff);
        Kr[2 * g + 1] = ldnt(kp + g * 2048 + 256 + koff);
      } else if constexpr (mode == 2) {
        Kr[2 * g] = *reinterpret_cast<const u32x4*>(wp + (size_t)g * 64 * DM);
        Kr[2 * g + 1] = *reinterpret_cast<const u32x4*>(wp + (size_t)g * 64 * DM + DM);
      }
      SB();
    }
#define TR_STEP(HALF, CTRL, BIT) _Pragma("unroll") for (int i = 0; i < (HALF); ++i) { \
      const float lo_ = x[i], hi_ = x[i + (HALF)]; const float t1_ = lo_ + dpp<CTRL>(lo_), t2_ = hi_ + dpp<CTRL>(hi_); \
      x[i] = (c & (BIT)) ? t2_ : t1_; }
    TR_STEP(4, XOR8, 8) TR_STEP(2, XOR7, 4) TR_STEP(1, XOR2, 2)
#undef TR_STEP
    float sc = x[0] + dpp<XOR1>(x[0]);
    sc = rbf(sc) * C2;
    if (__any(sc > m)) {
      float pm = sc;
      pm = fmaxf(pm, dpp<ROR4>(pm)); pm = fmaxf(pm, dpp<XOR8>(pm));
      pm = x32_max(x16_max(pm));
      const float mn = fmaxf(m, pm);
      const float alpha = __builtin_amdgcn_exp2f(m - mn);
      m = mn; l *= alpha;
      const float a0 = dpp<0x00>(alpha), a1 = dpp<0xAA>(alpha);
#pragma unroll
      for (int k = 0; k < 4; ++k) { acc[0][k] *= a0; acc[1][k] *= a1; }
    }
    const float p = __builtin_amdgcn_exp2f(sc - m);
    l += p;
    if ((c & 1) == 0) pl[pw] = p;
    f32x4 ppv[4];
#pragma unroll
    for (int i2 = 0; i2 < 4; ++i2) ppv[i2] = *reinterpret_cast<const f32x4*>(pl + r2 * 16 + i2 * 4);
    SB();
#pragma unroll
    for (int i2 = 0; i2 < 4; ++i2) {
      const f32x4 pp = ppv[i2];
#pragma unroll
      for (int e = 0; e < 2; ++e) {
        const int i = 2 * i2 + e;
        const float p0 = pp[2 * e], p1 = pp[2 * e + 1];
#pragma unroll
        for (int k = 0; k < 4; ++k) {
          const f32x2 vv = {lo2f(Vr[i][k]), hi2f(Vr[i][k])};
          acc[0][k] = __builtin_elementwise_fma(f32x2{p0, p0}, vv, acc[0][k]);
          acc[1][k] = __builtin_elementwise_fma(f32x2{p1, p1}, vv, acc[1][k]);
        }
        asm volatile("" : "+v"(acc[0][0]), "+v"(acc[0][1]), "+v"(acc[0][2]), "+v"(acc[0][3]), "+v"(acc[1][0]), "+v"(acc[1][1]), "+v"(acc[1][2]), "+v"(acc[1][3]) :: "memory");
        if constexpr (mode == 0) Vr[i] = ldnt(vp + i * 1024 + voff);
        else if constexpr (mode == 2) Vr[i] = *reinterpret_cast<const u32x4*>(wp + (size_t)(4 + i2) * 64 * DM + e * DM);
        SB();
      }
    }
  };
  LAS unsigned* const ctr = (LAS unsigned*)(smem + L_EP + 12);
  auto claim = [&]() { unsigned v = 0; if (lane == 0) v = __hip_atomic_fetch_add(ctr, 1u, __ATOMIC_RELAXED, __HIP_MEMORY_SCOPE_WORKGROUP); return v; };

  int nxt = __builtin_amdgcn_readfirstlane(claim());
  {
    const unsigned nn = claim();
    tile(K, V, IC<0>{}, nxt);
    nxt = __builtin_amdgcn_readfirstlane(nn);
  }
  if (wave == 7) {
    asm volatile("s_waitcnt vmcnt(16)" ::: "memory");
    if (lane == 0) gstore(ws + WS_READY + bid * 8, epoch);
  }
  tile(KB, VB, IC<1>{}, 0);
  while (nxt < RDY_AT) {
    const unsigned nn = claim();
    tile(K, V, IC<0>{}, nxt);
    nxt = __builtin_amdgcn_readfirstlane(nn);
  }
  const unsigned char* rdyp = ws + WS_READY + (wave * 32 + c2) * 8;
  u64 rdy = gload(rdyp);
  SB();
  while (nxt < NTW) {
    const unsigned nn = claim();
    tile(K, V, IC<0>{}, nxt);
    nxt = __builtin_amdgcn_readfirstlane(nn);
  }
  tile(K, V, IC<2>{}, 0);
  STAMP(6);
  if (!__all(rdy == epoch)) {
    unsigned spins = 0;
    do {
      __builtin_amdgcn_s_sleep(1);
      rdy = gload(rdyp);
    } while (!__all(rdy == epoch) && ++spins < SPIN_CAP);
  }
#pragma unroll
  for (int h = 0; h < 2; ++h)
#pragma unroll
    for (int k = 0; k < 4; ++k) { acc[h][k][0] = x32_sum(acc[h][k][0]); acc[h][k][1] = x32_sum(acc[h][k][1]); }
  l += dpp<ROR4>(l); l += dpp<XOR8>(l); l = x32_sum(x16_sum(l));
  float* mrg = reinterpret_cast<float*>(smem + L_MRG);
  if (lane < 32) {
#pragma unroll
    for (int h = 0; h < 2; ++h) {
      float* rec = mrg + (wave * 2 + h) * 260;
      *reinterpret_cast<f32x4*>(rec + 4 + c2 * 8) = f32x4{acc[h][0][0], acc[h][0][1], acc[h][1][0], acc[h][1][1]};
      *reinterpret_cast<f32x4*>(rec + 8 + c2 * 8) = f32x4{acc[h][2][0], acc[h][2][1], acc[h][3][0], acc[h][3][1]};
    }
    if ((lane & 29) == 0) *reinterpret_cast<f32x2*>(mrg + (wave * 2 + (lane >> 1)) * 260) = f32x2{m, l};
  }
  __syncthreads();
  STAMP(7);
  if (bid == 0 && tid == 0) gstore(ws + WS_EPOCH, epoch);
  f32x4 on = {0.f, 0.f, 0.f, 0.f};
  float lse = 0.f;
  if (wave < 2) {
    f32x2 ml[NWV];
#pragma unroll
    for (int w = 0; w < NWV; ++w) ml[w] = *reinterpret_cast<const f32x2*>(mrg + (w * 2 + wave) * 260);
    float M = ml[0][0];
#pragma unroll
    for (int w = 1; w < NWV; ++w) M = fmaxf(M, ml[w][0]);
    float L = 0.f;
#pragma unroll
    for (int w = 0; w < NWV; ++w) {
      const float wt = __builtin_amdgcn_exp2f(ml[w][0] - M);
      L = fmaf(wt, ml[w][1], L);
      on += wt * *reinterpret_cast<const f32x4*>(mrg + (w * 2 + wave) * 260 + 4 + lane * 4);
    }
    on *= 1.f / L;
    lse = M + __builtin_amdgcn_logf(L);
    const u32x4 ob = {pubbits(on[0]), pubbits(on[1]), pubbits(on[2]), pubbits(on[3])};
    __builtin_amdgcn_raw_buffer_store_b128(ob, wsr, prec + tid * 16, 0, SC1);
    if (lane == 0) __builtin_amdgcn_raw_buffer_store_b32(pubbits(lse), wsr, prec + 2048 + wave * 4, 0, SC1);
  }
  STAMP(8);
  constexpr float KC = 0.7978845608028654f, L2E = 1.4426950408889634f;
  constexpr float K1 = -2.f * KC * L2E, K2 = -2.f * KC * 0.044715f * L2E;
  float c0[7], c1[7], c2c[7], c3[7], a0c[7], a1c[7];
  u32x4 WL[8], WH[8];
#pragma unroll
  for (int mm = 0; mm < 8; ++mm) {
    const u32x4 wa = mm < 4 ? K[2 * mm] : V[2 * (mm - 4)], wb = mm < 4 ? K[2 * mm + 1] : V[2 * (mm - 4) + 1];
#pragma unroll
    for (int k = 0; k < 4; ++k) {
      WL[mm][k] = __builtin_amdgcn_perm(wb[k], wa[k], 0x05040100u);
      WH[mm][k] = __builtin_amdgcn_perm(wb[k], wa[k], 0x07060302u);
    }
    asm volatile("" : "+v"(WL[mm]), "+v"(WH[mm]));
  }
  {
    const u16* mw = reinterpret_cast<const u16*>(smem + L_MW);
#pragma unroll
    for (int i = 0; i < 7; ++i) {
      const int f = (tid & 255) + 256 * i;
      const float w = lo2f(mw[f]), bb = lo2f(mw[FF + f]), v = lo2f(mw[2 * FF + f]);
      c3[i] = K2 * w * w * w; c2c[i] = 3.f * K2 * w * w * bb; c1[i] = K1 * w + 3.f * K2 * w * bb * bb; c0[i] = K1 * bb + K2 * bb * bb * bb;
      a1c[i] = w * v; a0c[i] = bb * v;
      asm volatile("" : "+v"(c0[i]), "+v"(c1[i]), "+v"(c2c[i]), "+v"(c3[i]), "+v"(a0c[i]), "+v"(a1c[i]));
    }
  }
  if (wave < 2) {
    u32x4 sa[3]; unsigned sl[3];
    unsigned spins = 0;
    for (;;) {
      asm volatile("" ::: "memory");
      bool ok = true;
#pragma unroll
      for (int t = 0; t < 3; ++t) {
        const unsigned rec = (unsigned)WS_P + (unsigned)(((s + 1 + t) & 3) * 64 + (bid & 63)) * PREC * 4;
        sa[t] = __builtin_amdgcn_raw_buffer_load_b128(wsr, tid * 16, rec, SC1);
        sl[t] = __builtin_amdgcn_raw_buffer_load_b32(wsr, 2048 + wave * 4, rec, SC1);
      }
#pragma unroll
      for (int t = 0; t < 3; ++t) ok &= full4(sa[t]) && sl[t] != EMPTY;
      if (__all(ok)) break;
      __builtin_amdgcn_s_sleep(SPIN_SLEEP);
      if (++spins > SPIN_CAP) break;
    }
    const float l1 = __uint_as_float(sl[0]), l2 = __uint_as_float(sl[1]), l3 = __uint_as_float(sl[2]);
    const float MM = fmaxf(fmaxf(lse, l1), fmaxf(l2, l3));
    const float e0 = __builtin_amdgcn_exp2f(lse - MM), e1 = __builtin_amdgcn_exp2f(l1 - MM), e2 = __builtin_amdgcn_exp2f(l2 - MM), e3 = __builtin_amdgcn_exp2f(l3 - MM);
    const float inv = 1.f / ((e0 + e1) + (e2 + e3));
    const f32x4 oo = (e0 * on + e1 * __builtin_bit_cast(f32x4, sa[0]) + e2 * __builtin_bit_cast(f32x4, sa[1]) + e3 * __builtin_bit_cast(f32x4, sa[2])) * inv;
    typedef unsigned u32x2 __attribute__((ext_vector_type(2)));
    *reinterpret_cast<u32x2*>(smem + L_OB + tid * 8) = u32x2{bfbits(oo[0]) | (bfbits(oo[1]) << 16), bfbits(oo[2]) | (bfbits(oo[3]) << 16)};
  }
  STAMP(9);
  __syncthreads();
  {
    float ya[8];
#pragma unroll
    for (int k = 0; k < 8; ++k) ya[k] = 0.f;
#pragma unroll
    for (int mm = 0; mm < 8; ++mm) {
      const unsigned op = *reinterpret_cast<const unsigned*>(smem + L_OB + (64 * mm + 8 * wave + 2 * r) * 2);
#pragma unroll
      for (int k = 0; k < 4; ++k) { ya[2 * k] = dot2(WL[mm][k], op, ya[2 * k]); ya[2 * k + 1] = dot2(WH[mm][k], op, ya[2 * k + 1]); }
    }
#pragma unroll
    for (int k = 0; k < 8; ++k) ya[k] = x32_sum(x16_sum(ya[k]));
    float* red = reinterpret_cast<float*>(smem + L_RED2);
    if (lane < 14) {
      *reinterpret_cast<f32x4*>(red + wave * 128 + lane * 8) = f32x4{ya[0], ya[1], ya[2], ya[3]};
      *reinterpret_cast<f32x4*>(red + wave * 128 + lane * 8 + 4) = f32x4{ya[4], ya[5], ya[6], ya[7]};
    }
    STAMP(10);
    __syncthreads();
    if (tid < 112) {
      float t = 0.f;
#pragma unroll
      for (int w = 0; w < NWV; ++w) t += red[w * 128 + tid];
      __builtin_amdgcn_raw_buffer_store_b32(pubbits(t), wsr, yout + tid * 32, 0, SC1);
    }
  }
  STAMP(11);
  float* hs = reinterpret_cast<float*>(smem + L_HS);
  if (wave < 7) {
    const float xres = lo2f(reinterpret_cast<const u16*>(smem + L_XR)[tid]);
    const unsigned yin = (unsigned)WS_Y + (unsigned)b * DM * 32;
    u32x4 y0, y1;
    unsigned spins = 0;
    for (;;) {
      asm volatile("" ::: "memory");
      y0 = __builtin_amdgcn_raw_buffer_load_b128(wsr, tid * 32, yin, SC1);
      y1 = __builtin_amdgcn_raw_buffer_load_b128(wsr, tid * 32 + 16, yin, SC1);
      if (__all(full4(y0) && full4(y1))) break;
      __builtin_amdgcn_s_sleep(SPIN_SLEEP);
      if (++spins > SPIN_CAP) break;
    }
    const f32x4 f0 = __builtin_bit_cast(f32x4, y0), f1 = __builtin_bit_cast(f32x4, y1);
    const float t = ((f0[0] + f0[1]) + (f0[2] + f0[3])) + ((f1[0] + f1[1]) + (f1[2] + f1[3]));
    hs[tid] = rbf(xres + rbf(t));
  }
  STAMP(12);
  __syncthreads();
  const int jo = kvh * 4 + s;
  float v[7];
#pragma unroll
  for (int i = 0; i < 7; ++i) v[i] = hs[lane + 64 * i];
#pragma unroll
  for (int rep = 0; rep < 2; ++rep) {
    float sm = 0.f;
#pragma unroll
    for (int i = 0; i < 7; ++i) sm += v[i];
    const float mean = wave_sum(sm) * (1.f / DM);
    float vs = 0.f;
#pragma unroll
    for (int i = 0; i < 7; ++i) { const float d = v[i] - mean; vs += d * d; }
    const float var = wave_sum(vs) * (1.f / DM);
    const float mb = rbf(mean), rden = 1.f / rbf(sqrtf(rbf(rbf(var) + 1e-5f)));
#pragma unroll
    for (int i = 0; i < 7; ++i) v[i] = rbf(rbf(v[i] - mb) * rden);
  }
  STAMP(13);
  const int par = wave >> 2;
  float ma[7];
#pragma unroll
  for (int mo = 0; mo < 7; ++mo) {
    const int d = 14 * jo + 2 * mo + par;
    float hv = 0.f;
#pragma unroll
    for (int i = 0; i < 7; ++i) if ((d >> 6) == i) hv = v[i];
    const float h1 = __builtin_bit_cast(float, __builtin_amdgcn_readlane(__builtin_bit_cast(int, hv), d & 63));
    const float h2 = h1 * h1, h3 = h2 * h1;
    float a = 0.f;
#pragma unroll
    for (int i = 0; i < 7; ++i) {
      const float z = fmaf(c3[i], h3, fmaf(c2c[i], h2, fmaf(c1[i], h1, c0[i])));
      const float e = __builtin_amdgcn_exp2f(z);
      const float rr = __builtin_amdgcn_rcpf(1.f + e);
      a = fmaf(fmaf(a1c[i], h1, a0c[i]), rr, a);
    }
    ma[mo] = a;
  }
  asm volatile("" : "+v"(ma[0]), "+v"(ma[1]), "+v"(ma[2]), "+v"(ma[3]), "+v"(ma[4]), "+v"(ma[5]), "+v"(ma[6]));
  STAMP(14);
#pragma unroll
  for (int mo = 0; mo < 7; ++mo) ma[mo] = wave_sum(ma[mo]);
  float* red3 = reinterpret_cast<float*>(smem + L_RED3);
  if (lane == 0) {
#pragma unroll
    for (int mo = 0; mo < 7; ++mo) red3[wave * 8 + mo] = ma[mo];
  }
  __syncthreads();
  if (tid < 14) {
    const int mo = tid >> 1, pr = tid & 1;
    const float y = (red3[(pr * 4 + 0) * 8 + mo] + red3[(pr * 4 + 1) * 8 + mo]) + (red3[(pr * 4 + 2) * 8 + mo] + red3[(pr * 4 + 3) * 8 + mo]);
    const int d = 14 * jo + tid;
    const float y2 = rbf(rbf(y) + lo2f(*reinterpret_cast<const unsigned*>(smem + L_EP + 8)));
    prm.out[b * DM + d] = (u16)bfbits(hs[d] + y2);
  }
  STAMP(15);
}
}

extern "C" void kernel_launch(void* const* d_in, const int* in_sizes, int n_in, void* d_out, int out_size, void* d_ws, size_t ws_size,
                              hipStream_t stream) {
  Params p;
  p.x = (const u16*)d_in[0]; p.kc = (const u16*)d_in[1]; p.vc = (const u16*)d_in[2]; p.wq = (const u16*)d_in[3]; p.wo = (const u16*)d_in[4];
  p.w1 = (const u16*)d_in[6]; p.b1 = (const u16*)d_in[7]; p.w2 = (const u16*)d_in[8]; p.b2 = (const u16*)d_in[9];
  p.out = (u16*)d_out; p.ws = (unsigned char*)d_ws;
  k_fused<<<NWG, NT, 0, stream>>>(p);
}
```

```cpp
#include <hip/hip_runtime.h>
#include <hip/hip_bf16.h>
#include <stdint.h>
#include <type_traits>

#define STAMPS 0

namespace {
constexpr int NB = 8, DM = 448, HQ = 16, HKV = 8, SK = 8192, DH = 256, FF = 1792;
constexpr int NWG = 256, NT = 512, NWV = 8;
constexpr int NSPLIT = 4;
constexpr int TK = 16;
constexpr int NTILE = SK / TK / (NSPLIT * NWV);

typedef unsigned u32x4 __attribute__((ext_vector_type(4)));
typedef float f32x4 __attribute__((ext_vector_type(4)));
typedef float f32x2 __attribute__((ext_vector_type(2)));
typedef __bf16 bf16x2 __attribute__((ext_vector_type(2)));
typedef __bf16 bf16x8 __attribute__((ext_vector_type(8)));
typedef short s16x4 __attribute__((ext_vector_type(4)));
typedef short s16x8 __attribute__((ext_vector_type(8)));
typedef unsigned short u16;
typedef unsigned long long u64;
#define LAS __attribute__((address_space(3)))
#define GAS __attribute__((address_space(1)))
typedef GAS u64 gu64;
#define RLX_AGENT __ATOMIC_RELAXED, __HIP_MEMORY_SCOPE_AGENT

__device__ __forceinline__ float rbf(float x) { return (float)(__bf16)x; }
__device__ __forceinline__ unsigned bfbits(float x) { return (unsigned)__builtin_bit_cast(u16, (__bf16)x); }
__device__ __forceinline__ float lo2f(unsigned u) { return __builtin_bit_cast(float, u << 16); }
__device__ __forceinline__ float hi2f(unsigned u) { return __builtin_bit_cast(float, u & 0xffff0000u); }
__device__ __forceinline__ float dot2(unsigned k, unsigned q, float acc) {
  return __builtin_amdgcn_fdot2_f32_bf16(__builtin_bit_cast(bf16x2, k), __builtin_bit_cast(bf16x2, q), acc, false);
}
template <int CTRL> __device__ __forceinline__ float dpp(float x) {
  return __builtin_bit_cast(float, __builtin_amdgcn_mov_dpp(__builtin_bit_cast(int, x), CTRL, 0xf, 0xf, true));
}
constexpr int XOR1 = 0xB1, XOR2 = 0x4E, XOR7 = 0x141, XOR8 = 0x128, ROR4 = 0x124;
__device__ __forceinline__ float x16_sum(float x) {
  auto s = __builtin_amdgcn_permlane16_swap(__float_as_uint(x), __float_as_uint(x), false, false);
  return __uint_as_float(s[0]) + __uint_as_float(s[1]);
}
__device__ __forceinline__ float x32_sum(float x) {
  auto t = __builtin_amdgcn_permlane32_swap(__float_as_uint(x), __float_as_uint(x), false, false);
  return __uint_as_float(t[0]) + __uint_as_float(t[1]);
}
__device__ __forceinline__ float x16_max(float x) {
  auto s = __builtin_amdgcn_permlane16_swap(__float_as_uint(x), __float_as_uint(x), false, false);
  return fmaxf(__uint_as_float(s[0]), __uint_as_float(s[1]));
}
__device__ __forceinline__ float x32_max(float x) {
  auto t = __builtin_amdgcn_permlane32_swap(__float_as_uint(x), __float_as_uint(x), false, false);
  return fmaxf(__uint_as_float(t[0]), __uint_as_float(t[1]));
}
__device__ __forceinline__ float wave_sum(float x) {
  x += dpp<XOR1>(x); x += dpp<XOR2>(x); x += dpp<XOR7>(x); x += dpp<XOR8>(x);
  return x32_sum(x16_sum(x));
}
__device__ __forceinline__ u32x4 ldnt(const void* p) { return __builtin_nontemporal_load(reinterpret_cast<const u32x4*>(p)); }
__device__ __forceinline__ u64 gload(const unsigned char* p) { return __hip_atomic_load((gu64*)p, RLX_AGENT); }
__device__ __forceinline__ void gstore(unsigned char* p, u64 v) { __hip_atomic_store((gu64*)p, v, RLX_AGENT); }
#define SB() __builtin_amdgcn_sched_barrier(0)

constexpr size_t WS_EPOCH = 0;
constexpr size_t WS_READY = 4096;
constexpr size_t WS_Q = 8192;
constexpr int PREC = 576;
constexpr size_t WS_P = WS_Q + (size_t)64 * 4 * 512 * 8;
constexpr size_t WS_Y = WS_P + (size_t)NWG * PREC * 4;
constexpr size_t WS_END = WS_Y + (size_t)NB * 8 * DM * 4;
constexpr size_t WS_STAMP = (size_t)16 << 20;
constexpr unsigned SPIN_CAP = 1u << 18;
constexpr unsigned EMPTY = 0xffffffffu;
constexpr int NTW = SK / TK / NSPLIT;

constexpr int L_WSL = 0;
constexpr int L_RED = L_WSL + 8 * 2048;
constexpr int L_QB = L_RED + 4 * 4096;
constexpr int L_PL = L_QB + 1024;
constexpr int L_MRG = L_PL + 1024;
constexpr int L_OB = L_MRG + NWV * 2 * 260 * 4;
constexpr int L_RED2 = L_OB + 2048;
constexpr int L_HS = L_RED2 + 8192;
constexpr int L_RED3 = L_HS + 1792;
constexpr int L_MW = L_RED3 + 256;
constexpr int L_XR = L_MW + 3 * FF * 2;
constexpr int L_EP = L_XR + 896;
constexpr int L_ST = L_EP + 16;
constexpr int L_QF = L_ST + 192;
constexpr int L_DUMMY = L_QF + 16;
constexpr int L_END = L_DUMMY + 16;
constexpr unsigned OOB = 0x40000000u;
#define POLL_DELAY 8
#define SPIN_SLEEP 2
#define RDY_AT 80

struct Params {
  const u16* x; const u16* kc; const u16* vc; const u16* wq; const u16* wo;
  const u16* w1; const u16* b1; const u16* w2; const u16* b2;
  u16* out; unsigned char* ws;
};

#define STAMP(i) do {} while (0)
typedef unsigned u32x2 __attribute__((ext_vector_type(2)));
__device__ __forceinline__ unsigned pubbits(float v) { return min(__float_as_uint(v), 0xfffffffeu); }
__device__ __forceinline__ bool full4(u32x4 v) { return max(max(v[0], v[1]), max(v[2], v[3])) != EMPTY; }
__device__ __forceinline__ float ln_rden(float var) { return __builtin_amdgcn_rcpf(rbf(__builtin_amdgcn_sqrtf(rbf(rbf(var) + 1e-5f)))); }
template <int N> using IC = std::integral_constant<int, N>;

__global__ void __launch_bounds__(NT, 2) k_fused(Params prm) {
  __shared__ __attribute__((aligned(16))) unsigned char smem[L_END];
  unsigned char* const ws = prm.ws;
  const int tid = threadIdx.x, lane = tid & 63, wave = __builtin_amdgcn_readfirstlane(tid >> 6);
  const int bid = blockIdx.x, kvh = bid & 7, b = (bid >> 3) & 7, s = bid >> 6, jq = bid >> 3;
  const int r = lane >> 4, c = lane & 15, r2 = lane >> 5, c2 = lane & 31;
  STAMP(0);
  const __amdgpu_buffer_rsrc_t wsr = __builtin_amdgcn_make_buffer_rsrc(ws, 0, (int)WS_END, 0x00020000);
  constexpr int SC1 = 16;
  const unsigned prec = (unsigned)WS_P + (unsigned)bid * PREC * 4;
  const int bg = jq / 7, cq = jq - bg * 7;
  const unsigned yout = (unsigned)WS_Y + (unsigned)(((2 * bg) * 8 + kvh) * DM + cq * 64) * 4;
  const int rg = jq >> 3, cg = jq & 7;
  u32x4 xv[2], wv[2][2]; u32x2 xa[2]; u64 ep0;
  if (wave < 4) {
#pragma unroll
    for (int e = 0; e < 2; ++e) {
      const int ks = (wave + 4 * e) < 7 ? wave + 4 * e : 6;
      xv[e] = ldnt(prm.x + (wave + 4 * e) * DM + (lane < 56 ? lane : 55) * 8);
      xa[e] = __builtin_nontemporal_load(reinterpret_cast<const u32x2*>(prm.x + (c & 7) * DM + rg * 112 + 16 * ks + 4 * r));
      const u16* wqp = prm.wq + (size_t)(rg * 112 + 16 * ks + (lane >> 3)) * 4096 + kvh * 512 + cg * 64 + (lane & 7) * 8;
      wv[e][0] = ldnt(wqp);
      wv[e][1] = ldnt(wqp + 8 * 4096);
    }
    ep0 = gload(ws + WS_EPOCH);
  }
  *reinterpret_cast<u32x2*>(smem + L_QF) = u32x2{0u, 0u};
  SB();
  __syncthreads();
  SB();
  u64 epoch = 0;
  LAS unsigned* const qf = (LAS unsigned*)(smem + L_QF);
  if (wave < 4) {
    auto qsync = [&](unsigned target) {
      asm volatile("s_waitcnt lgkmcnt(0)" ::: "memory");
      if (lane == 0) __hip_atomic_fetch_add(qf + 1, 1u, __ATOMIC_RELAXED, __HIP_MEMORY_SCOPE_WORKGROUP);
      while (__hip_atomic_load(qf + 1, __ATOMIC_RELAXED, __HIP_MEMORY_SCOPE_WORKGROUP) < target) {}
      asm volatile("" ::: "memory");
    };
    u16* wsl = reinterpret_cast<u16*>(smem + L_WSL) + wave * 2048;
    float* red = reinterpret_cast<float*>(smem + L_RED);
    STAMP(1);
#pragma unroll
    for (int e = 0; e < 2; ++e) {
      *reinterpret_cast<u32x4*>(wsl + e * 1024 + (lane >> 3) * 64 + (lane & 7) * 8) = wv[e][0];
      *reinterpret_cast<u32x4*>(wsl + e * 1024 + (8 + (lane >> 3)) * 64 + (lane & 7) * 8) = wv[e][1];
    }
#pragma unroll
    for (int e = 0; e < 2; ++e) {
      float xf[8];
#pragma unroll
      for (int k = 0; k < 4; ++k) { xf[2 * k] = lo2f(xv[e][k]); xf[2 * k + 1] = hi2f(xv[e][k]); }
      float sm = 0.f;
#pragma unroll
      for (int k = 0; k < 8; ++k) sm += xf[k];
      if (lane >= 56) sm = 0.f;
      const float mean = wave_sum(sm) * (1.f / DM);
      float vs = 0.f;
#pragma unroll
      for (int k = 0; k < 8; ++k) { const float d = xf[k] - mean; vs += d * d; }
      if (lane >= 56) vs = 0.f;
      const float var = wave_sum(vs) * (1.f / DM);
      *reinterpret_cast<f32x2*>(smem + L_ST + (wave + 4 * e) * 8) = f32x2{rbf(mean), ln_rden(var)};
      *reinterpret_cast<u32x4*>(smem + ((wave + 4 * e == b && lane < 56) ? L_XR + lane * 16 : L_DUMMY)) = xv[e];
    }
    s16x4 bfr[2][4];
#pragma unroll
    for (int e = 0; e < 2; ++e)
#pragma unroll
      for (int ct = 0; ct < 4; ++ct) bfr[e][ct] = __builtin_amdgcn_ds_read_tr16_b64_v4i16((LAS s16x4*)(wsl + e * 1024 + (4 * r + (c >> 2)) * 64 + ct * 16 + (c & 3) * 4));
    asm volatile("" : "+v"(ep0));
    epoch = ep0 + 1ull;
    *reinterpret_cast<u64*>(smem + L_EP) = epoch;
    STAMP(2);
    qsync(4);
    {
      const f32x2 st = *reinterpret_cast<const f32x2*>(smem + L_ST + (c & 7) * 8);
      u32x2 av[2];
#pragma unroll
      for (int e = 0; e < 2; ++e)
#pragma unroll
        for (int k = 0; k < 2; ++k) av[e][k] = bfbits(rbf(lo2f(xa[e][k]) - st[0]) * st[1]) | (bfbits(rbf(hi2f(xa[e][k]) - st[0]) * st[1]) << 16);
      if (wave == 3) av[1] = u32x2{0u, 0u};
#pragma unroll
      for (int ct = 0; ct < 4; ++ct) {
        f32x4 d = __builtin_amdgcn_mfma_f32_16x16x16bf16_1k(__builtin_bit_cast(s16x4, av[0]), bfr[0][ct], f32x4{0.f, 0.f, 0.f, 0.f}, 0, 0, 0);
        d = __builtin_amdgcn_mfma_f32_16x16x16bf16_1k(__builtin_bit_cast(s16x4, av[1]), bfr[1][ct], d, 0, 0, 0);
        float* o = red + wave * 1024 + 4 * r * 64 + ct * 16 + c;
        o[0] = d[0]; o[64] = d[1]; o[128] = d[2]; o[192] = d[3];
      }
    }
    qsync(8);
    STAMP(3);
#pragma unroll
    for (int e = 0; e < 2; ++e) {
      const float* rp = red + (wave + 4 * e) * 64 + lane;
      const float S = (rp[0] + rp[1024]) + (rp[2048] + rp[3072]);
      gstore(ws + WS_Q + ((size_t)((kvh * 8 + wave + 4 * e) * 4 + rg) * 512 + cg * 64 + lane) * 8, (epoch << 24) | (u64)((__float_as_uint(S) + 0x80u) >> 8));
    }
    SB();
    qsync(12);
    STAMP(4);
    if (wave == 0) {
      const unsigned qin = (unsigned)WS_Q + (unsigned)(kvh * 8 + b) * (4 * 512 * 8);
      const unsigned wlo = (unsigned)(epoch << 24), whi = (unsigned)(epoch >> 8);
      u32x4 g[16];
      unsigned spins = 0;
      __builtin_amdgcn_s_sleep(POLL_DELAY);
      for (;;) {
        asm volatile("" ::: "memory");
#pragma unroll
        for (int i = 0; i < 16; ++i) g[i] = __builtin_amdgcn_raw_buffer_load_b128(wsr, lane * 16 + i * 1024, qin, SC1);
        unsigned bad = 0;
#pragma unroll
        for (int i = 0; i < 16; ++i) bad |= ((g[i][0] ^ wlo) & 0xff000000u) | (g[i][1] ^ whi) | ((g[i][2] ^ wlo) & 0xff000000u) | (g[i][3] ^ whi);
        if (__all(bad == 0)) break;
        __builtin_amdgcn_s_sleep(1);
        if (++spins > SPIN_CAP) break;
      }
#pragma unroll
      for (int j = 0; j < 4; ++j) {
        float q0 = 0.f, q1 = 0.f;
#pragma unroll
        for (int k4 = 0; k4 < 4; ++k4) { q0 += __uint_as_float(g[k4 * 4 + j][0] << 8); q1 += __uint_as_float(g[k4 * 4 + j][2] << 8); }
        *reinterpret_cast<unsigned*>(smem + L_QB + (j * 128 + 2 * lane) * 2) = bfbits(q0) | (bfbits(q1) << 16);
      }
      __hip_atomic_store(qf, 1u, __ATOMIC_RELEASE, __HIP_MEMORY_SCOPE_WORKGROUP);
    }
  }
  const size_t pair_off = (size_t)(b * HKV + kvh) * SK * DH * 2 + (size_t)s * NWV * (TK * DH * 2);
  const unsigned char* const kbase = reinterpret_cast<const unsigned char*>(prm.kc) + pair_off;
  const unsigned char* const vbase = reinterpret_cast<const unsigned char*>(prm.vc) + pair_off;
  auto tile_off = [](int T) { return (size_t)(unsigned)(((T >> 3) * 32 + (T & 7)) * (TK * DH * 2)); };
  const unsigned koff = r * 512 + c * 16, voff = lane * 16;
  u32x4 K[8], V[8], KB[8], VB[8];
  {
    const unsigned char* kp = kbase + tile_off(wave);
    const unsigned char* vp = vbase + tile_off(wave);
#pragma unroll
    for (int i = 0; i < 8; ++i) K[i] = ldnt(kp + (i >> 1) * 2048 + (i & 1) * 256 + koff);
#pragma unroll
    for (int i = 0; i < 8; ++i) V[i] = ldnt(vp + i * 1024 + voff);
    SB();
    if (wave == 7) {
      const u32x4 e4 = {EMPTY, EMPTY, EMPTY, EMPTY};
      __builtin_amdgcn_raw_buffer_store_b128(e4, wsr, prec + lane * 16, 0, SC1);
      __builtin_amdgcn_raw_buffer_store_b128(e4, wsr, prec + 1024 + lane * 16, 0, SC1);
      const unsigned yo = yout + (lane >= 32 ? 8 * DM * 4 : 0) + (lane & 15) * 16;
      __builtin_amdgcn_raw_buffer_store_b128(e4, wsr, lane < 16 ? prec + 2048 + lane * 16 : ((lane < 48 && jq < 28) ? yo : OOB), 0, SC1);
    }
    SB();
    kp = kbase + tile_off(NWV + wave);
    vp = vbase + tile_off(NWV + wave);
#pragma unroll
    for (int i = 0; i < 8; ++i) KB[i] = ldnt(kp + (i >> 1) * 2048 + (i & 1) * 256 + koff);
#pragma unroll
    for (int i = 0; i < 8; ++i) VB[i] = ldnt(vp + i * 1024 + voff);
    SB();
  }
  while (__hip_atomic_load(qf, __ATOMIC_ACQUIRE, __HIP_MEMORY_SCOPE_WORKGROUP) == 0u) __builtin_amdgcn_s_sleep(1);
  epoch = *reinterpret_cast<const u64*>(smem + L_EP);
  u32x4 q[2][2];
#pragma unroll
  for (int h = 0; h < 2; ++h)
#pragma unroll
    for (int j = 0; j < 2; ++j) q[h][j] = *reinterpret_cast<const u32x4*>(smem + L_QB + (h * 256 + (c + 16 * j) * 8) * 2);
  STAMP(5);

  float m = -INFINITY, l = 0.f;
  f32x2 acc[2][4];
#pragma unroll
  for (int h = 0; h < 2; ++h)
#pragma unroll
    for (int k = 0; k < 4; ++k) acc[h][k] = f32x2{0.f, 0.f};
  float* pl = reinterpret_cast<float*>(smem + L_PL) + wave * 32;
  const int pw = (r & 1) * 16 + ((c >> 2) * 2 + (r >> 1)) * 2 + ((c >> 1) & 1);
  constexpr float C2 = 0.0625f * 1.4426950408889634f;
  const u16* wp = prm.wo + (size_t)(kvh * 512 + 64 * wave + 32 * ((lane >> 3) & 1) + 8 * r) * DM + (cq < 7 && jq < 28 ? cq : 0) * 64 + (lane & 7) * 8;

  auto tile = [&](u32x4 (&Kr)[8], u32x4 (&Vr)[8], auto MODE, int T) {
    constexpr int mode = decltype(MODE)::value;
    const unsigned char* kp = kbase + tile_off(T);
    const unsigned char* vp = vbase + tile_off(T);
    float x[8];
#pragma unroll
    for (int g = 0; g < 4; ++g) {
#pragma unroll
      for (int h = 0; h < 2; ++h) {
        float d = 0.f;
#pragma unroll
        for (int j = 0; j < 2; ++j)
#pragma unroll
          for (int k = 0; k < 4; ++k) d = dot2(Kr[2 * g + j][k], q[h][j][k], d);
        x[2 * g + h] = d;
      }
      if constexpr (mode == 0) {
        Kr[2 * g] = ldnt(kp + g * 2048 + koff);
        Kr[2 * g + 1] = ldnt(kp + g * 2048 + 256 + koff);
      } else if constexpr (mode == 2) {
        Kr[2 * g] = ldnt(wp + (2 * g) * DM);
        Kr[2 * g + 1] = ldnt(wp + (2 * g + 1) * DM);
      }
      SB();
    }
#define TR_STEP(HALF, CTRL, BIT) _Pragma("unroll") for (int i = 0; i < (HALF); ++i) { \
      const float lo_ = x[i], hi_ = x[i + (HALF)]; const float t1_ = lo_ + dpp<CTRL>(lo_), t2_ = hi_ + dpp<CTRL>(hi_); \
      x[i] = (c & (BIT)) ? t2_ : t1_; }
    TR_STEP(4, XOR8, 8) TR_STEP(2, XOR7, 4) TR_STEP(1, XOR2, 2)
#undef TR_STEP
    float sc = x[0] + dpp<XOR1>(x[0]);
    sc = rbf(sc) * C2;
    if (__any(sc > m)) {
      float pm = sc;
      pm = fmaxf(pm, dpp<ROR4>(pm)); pm = fmaxf(pm, dpp<XOR8>(pm));
      pm = x32_max(x16_max(pm));
      const float mn = fmaxf(m, pm);
      const float alpha = __builtin_amdgcn_exp2f(m - mn);
      m = mn; l *= alpha;
      const float a0 = dpp<0x00>(alpha), a1 = dpp<0xAA>(alpha);
#pragma unroll
      for (int k = 0; k < 4; ++k) { acc[0][k] *= a0; acc[1][k] *= a1; }
    }
    const float p = __builtin_amdgcn_exp2f(sc - m);
    l += p;
    pl[pw] = p;
    f32x4 ppv[4];
#pragma unroll
    for (int i2 = 0; i2 < 4; ++i2) ppv[i2] = *reinterpret_cast<const f32x4*>(pl + r2 * 16 + i2 * 4);
    SB();
#pragma unroll
    for (int i2 = 0; i2 < 4; ++i2) {
      const f32x4 pp = ppv[i2];
#pragma unroll
      for (int e = 0; e < 2; ++e) {
        const int i = 2 * i2 + e;
        const float p0 = pp[2 * e], p1 = pp[2 * e + 1];
#pragma unroll
        for (int k = 0; k < 4; ++k) {
          const f32x2 vv = {lo2f(Vr[i][k]), hi2f(Vr[i][k])};
          acc[0][k] = __builtin_elementwise_fma(f32x2{p0, p0}, vv, acc[0][k]);
          acc[1][k] = __builtin_elementwise_fma(f32x2{p1, p1}, vv, acc[1][k]);
        }
        asm volatile("" : "+v"(acc[0][0]), "+v"(acc[0][1]), "+v"(acc[0][2]), "+v"(acc[0][3]), "+v"(acc[1][0]), "+v"(acc[1][1]), "+v"(acc[1][2]), "+v"(acc[1][3]) :: "memory");
        if constexpr (mode == 0) Vr[i] = ldnt(vp + i * 1024 + voff);
        SB();
      }
    }
  };
  tile(K, V, IC<0>{}, 2 * NWV + wave);
  if (wave == 7) {
    asm volatile("s_waitcnt vmcnt(16)" ::: "memory");
    __builtin_amdgcn_raw_buffer_store_b64(__builtin_bit_cast(u32x2, epoch), wsr, lane == 0 ? (unsigned)WS_READY + bid * 8 : OOB, 0, SC1);
  }
  tile(KB, VB, IC<2>{}, 0);
  u32x4 mw[3];
  const int mi = tid >= 256 && tid < 480 ? tid - 256 : 0;
  mw[0] = ldnt(prm.w1 + mi * 8);
  mw[1] = ldnt(prm.b1 + mi * 8);
  mw[2] = ldnt(prm.w2 + mi * 8);
  const unsigned b2r = __builtin_nontemporal_load(prm.b2);
  SB();
  tile(K, V, IC<0>{}, 3 * NWV + wave);
  u32x4 BF[8];
#pragma unroll
  for (int p = 0; p < 4; ++p)
#pragma unroll
    for (int k = 0; k < 4; ++k) {
      BF[2 * k][p] = __builtin_amdgcn_perm(KB[2 * p + 1][k], KB[2 * p][k], 0x05040100u);
      BF[2 * k + 1][p] = __builtin_amdgcn_perm(KB[2 * p + 1][k], KB[2 * p][k], 0x07060302u);
    }
#pragma unroll
  for (int j = 0; j < 8; ++j) asm volatile("" : "+v"(BF[j]));
  *reinterpret_cast<unsigned*>(smem + L_EP + 8) = b2r;
#pragma unroll
  for (int i = 0; i < 3; ++i) *reinterpret_cast<u32x4*>(smem + L_MW + i * FF * 2 + mi * 16) = mw[i];
  SB();
  int nxt = 4 * NWV + wave;
  for (; nxt < RDY_AT; nxt += NWV) tile(K, V, IC<0>{}, nxt);
  const unsigned char* rdyp = ws + WS_READY + (wave * 32 + c2) * 8;
  u64 rdy = gload(rdyp);
  SB();
  for (; nxt < NTW; nxt += NWV) tile(K, V, IC<0>{}, nxt);
  tile(K, V, IC<1>{}, 0);
  STAMP(6);
  if (!__all(rdy == epoch)) {
    unsigned spins = 0;
    do {
      __builtin_amdgcn_s_sleep(1);
      rdy = gload(rdyp);
    } while (!__all(rdy == epoch) && ++spins < SPIN_CAP);
  }
#pragma unroll
  for (int h = 0; h < 2; ++h)
#pragma unroll
    for (int k = 0; k < 4; ++k) { acc[h][k][0] = x32_sum(acc[h][k][0]); acc[h][k][1] = x32_sum(acc[h][k][1]); }
  l += dpp<ROR4>(l); l += dpp<XOR8>(l); l = x32_sum(x16_sum(l));
  float* mrg = reinterpret_cast<float*>(smem + L_MRG);
  {
    float* rec = mrg + (wave * 2 + r2) * 260;
    float a8[8];
#pragma unroll
    for (int k = 0; k < 4; ++k) {
      float u0 = acc[0][k][0], u1 = acc[0][k][1], w0 = acc[1][k][0], w1 = acc[1][k][1];
      asm volatile("" : "+v"(u0), "+v"(u1), "+v"(w0), "+v"(w1));
      a8[2 * k] = r2 ? w0 : u0; a8[2 * k + 1] = r2 ? w1 : u1;
    }
    *reinterpret_cast<f32x4*>(rec + 4 + c2 * 8) = f32x4{a8[0], a8[1], a8[2], a8[3]};
    *reinterpret_cast<f32x4*>(rec + 8 + c2 * 8) = f32x4{a8[4], a8[5], a8[6], a8[7]};
    *reinterpret_cast<f32x2*>(mrg + (wave * 2 + ((c >> 1) & 1)) * 260) = f32x2{m, l};
  }
  __syncthreads();
  STAMP(7);
  if (wave < 2) {
    f32x2 ml[NWV];
    f32x4 pa[NWV];
#pragma unroll
    for (int w = 0; w < NWV; ++w) { ml[w] = *reinterpret_cast<const f32x2*>(mrg + (w * 2 + wave) * 260); pa[w] = *reinterpret_cast<const f32x4*>(mrg + (w * 2 + wave) * 260 + 4 + lane * 4); }
    float M = ml[0][0];
#pragma unroll
    for (int w = 1; w < NWV; ++w) M = fmaxf(M, ml[w][0]);
    float L = 0.f;
    f32x4 on = {0.f, 0.f, 0.f, 0.f};
#pragma unroll
    for (int w = 0; w < NWV; ++w) {
      const float wt = __builtin_amdgcn_exp2f(ml[w][0] - M);
      L = fmaf(wt, ml[w][1], L);
      on += wt * pa[w];
    }
    on *= __builtin_amdgcn_rcpf(L);
    const float lse = M + __builtin_amdgcn_logf(L);
    const u32x4 ob = {pubbits(on[0]), pubbits(on[1]), pubbits(on[2]), pubbits(on[3])};
    __builtin_amdgcn_raw_buffer_store_b128(ob, wsr, prec + tid * 16, 0, SC1);
    __builtin_amdgcn_raw_buffer_store_b32(pubbits(lse), wsr, lane == 0 ? prec + 2048 + wave * 128 : OOB, 0, SC1);
  }
  if (bid == 0 && tid == 448) gstore(ws + WS_EPOCH, epoch);
  STAMP(8);
  constexpr float KC = 0.7978845608028654f, L2E = 1.4426950408889634f;
  constexpr float K1 = -2.f * KC * L2E, K2 = -2.f * KC * 0.044715f * L2E;
  f32x2 c0[4], c1[4], c2c[4], c3[4], a0c[4], a1c[4];
  float wmax = 0.f, bmax = 0.f;
  {
    const u16* mwp = reinterpret_cast<const u16*>(smem + L_MW);
#pragma unroll
    for (int i = 0; i < 7; ++i) {
      const int f = (tid & 255) + 256 * i;
      const float w = lo2f(mwp[f]), bb = lo2f(mwp[FF + f]), v = lo2f(mwp[2 * FF + f]);
      c3[i >> 1][i & 1] = K2 * w * w * w; c2c[i >> 1][i & 1] = 3.f * K2 * w * w * bb;
      c1[i >> 1][i & 1] = K1 * w + 3.f * K2 * w * bb * bb; c0[i >> 1][i & 1] = K1 * bb + K2 * bb * bb * bb;
      a1c[i >> 1][i & 1] = w * v; a0c[i >> 1][i & 1] = bb * v;
      wmax = fmaxf(wmax, fabsf(w)); bmax = fmaxf(bmax, fabsf(bb));
    }
    c0[3][1] = c1[3][1] = c2c[3][1] = c3[3][1] = a0c[3][1] = a1c[3][1] = 0.f;
#pragma unroll
    for (int i = 0; i < 4; ++i) asm volatile("" : "+v"(c0[i]), "+v"(c1[i]), "+v"(c2c[i]), "+v"(c3[i]), "+v"(a0c[i]), "+v"(a1c[i]));
    asm volatile("" : "+v"(wmax), "+v"(bmax));
  }
  if (jq < 28) {
    if (wave >= 4) {
      const int beta = (wave >> 1) & 1, h = wave & 1;
      u32x4 sa[4]; unsigned sl[4];
      unsigned spins = 0;
      for (;;) {
        asm volatile("" ::: "memory");
#pragma unroll
        for (int t = 0; t < 4; ++t) {
          const unsigned rec = (unsigned)WS_P + (unsigned)(t * 64 + (2 * bg + beta) * 8 + kvh) * PREC * 4;
          sa[t] = __builtin_amdgcn_raw_buffer_load_b128(wsr, h * 1024 + lane * 16, rec, SC1);
          sl[t] = __builtin_amdgcn_raw_buffer_load_b32(wsr, 2048 + h * 128, rec, SC1);
        }
        bool ok = true;
#pragma unroll
        for (int t = 0; t < 4; ++t) ok &= full4(sa[t]) && sl[t] != EMPTY;
        if (__all(ok)) break;
        __builtin_amdgcn_s_sleep(SPIN_SLEEP);
        if (++spins > SPIN_CAP) break;
      }
      const float l0 = __uint_as_float(sl[0]), l1 = __uint_as_float(sl[1]), l2 = __uint_as_float(sl[2]), l3 = __uint_as_float(sl[3]);
      const float MM = fmaxf(fmaxf(l0, l1), fmaxf(l2, l3));
      const float e0 = __builtin_amdgcn_exp2f(l0 - MM), e1 = __builtin_amdgcn_exp2f(l1 - MM), e2 = __builtin_amdgcn_exp2f(l2 - MM), e3 = __builtin_amdgcn_exp2f(l3 - MM);
      const float inv = __builtin_amdgcn_rcpf((e0 + e1) + (e2 + e3));
      const f32x4 oo = (e0 * __builtin_bit_cast(f32x4, sa[0]) + e1 * __builtin_bit_cast(f32x4, sa[1]) + e2 * __builtin_bit_cast(f32x4, sa[2]) + e3 * __builtin_bit_cast(f32x4, sa[3])) * inv;
      *reinterpret_cast<u32x2*>(smem + L_OB + (tid - 256) * 8) = u32x2{bfbits(oo[0]) | (bfbits(oo[1]) << 16), bfbits(oo[2]) | (bfbits(oo[3]) << 16)};
    }
    STAMP(9);
    __syncthreads();
    float* red = reinterpret_cast<float*>(smem + L_RED2);
    {
      const bf16x8 a = *reinterpret_cast<const bf16x8*>(smem + L_OB + (((c >> 1) & 1) * 512 + 64 * wave + 32 * (c & 1) + 8 * r) * 2);
      f32x4 y0[2], y1[2];
#pragma unroll
      for (int j = 0; j < 8; ++j) {
        const f32x4 d = __builtin_amdgcn_mfma_f32_16x16x32_bf16(a, __builtin_bit_cast(bf16x8, BF[j]), f32x4{0.f, 0.f, 0.f, 0.f}, 0, 0, 0);
        y0[j >> 2][j & 3] = (c & 8) ? d[1] : d[0];
        y1[j >> 2][j & 3] = (c & 8) ? d[3] : d[2];
      }
      float* o = red + (wave * 2 + (c >> 3)) * 128 + (c & 7) * 8;
      *reinterpret_cast<f32x4*>(o) = y0[0]; *reinterpret_cast<f32x4*>(o + 4) = y0[1];
      *reinterpret_cast<f32x4*>(o + 64) = y1[0]; *reinterpret_cast<f32x4*>(o + 68) = y1[1];
    }
    STAMP(10);
    __syncthreads();
    if (wave == 0) {
      f32x4 t = *reinterpret_cast<const f32x4*>(red + (r2 * 8) * 128 + c2 * 4);
#pragma unroll
      for (int w = 1; w < 8; ++w) t += *reinterpret_cast<const f32x4*>(red + (r2 * 8 + w) * 128 + c2 * 4);
      const u32x4 yb = {pubbits(x32_sum(t[0])), pubbits(x32_sum(t[1])), pubbits(x32_sum(t[2])), pubbits(x32_sum(t[3]))};
      __builtin_amdgcn_raw_buffer_store_b128(yb, wsr, lane < 32 ? yout + (lane >> 4) * (8 * DM * 4) + c * 16 : OOB, 0, SC1);
    }
  }
  STAMP(11);
  float* hs = reinterpret_cast<float*>(smem + L_HS);
  float hv = 0.f;
  if (wave < 7) {
    const float xres = lo2f(reinterpret_cast<const u16*>(smem + L_XR)[tid]);
    const unsigned yin = (unsigned)WS_Y + (unsigned)b * (8 * DM * 4);
    unsigned yv[8];
    unsigned spins = 0;
    for (;;) {
      asm volatile("" ::: "memory");
#pragma unroll
      for (int k = 0; k < 8; ++k) yv[k] = __builtin_amdgcn_raw_buffer_load_b32(wsr, tid * 4 + k * (DM * 4), yin, SC1);
      unsigned mx = yv[0];
#pragma unroll
      for (int k = 1; k < 8; ++k) mx = max(mx, yv[k]);
      if (__all(mx != EMPTY)) break;
      __builtin_amdgcn_s_sleep(SPIN_SLEEP);
      if (++spins > SPIN_CAP) break;
    }
    const float t = ((__uint_as_float(yv[0]) + __uint_as_float(yv[1])) + (__uint_as_float(yv[2]) + __uint_as_float(yv[3]))) +
                    ((__uint_as_float(yv[4]) + __uint_as_float(yv[5])) + (__uint_as_float(yv[6]) + __uint_as_float(yv[7])));
    hv = rbf(xres + rbf(t));
    hs[tid] = hv;
  }
  STAMP(12);
  const int jo = kvh * 4 + s, par = wave >> 2;
  float mbs[2], rds[2];
#pragma unroll
  for (int rep = 0; rep < 2; ++rep) {
    const float mwv = wave_sum(hv) * (1.f / 64.f);
    const float dv = hv - mwv;
    const float m2 = wave_sum(dv * dv);
    f32x2* stp = reinterpret_cast<f32x2*>(smem + L_ST + 64 + rep * 64);
    stp[wave] = f32x2{mwv, m2};
    __syncthreads();
    f32x2 sv[7];
#pragma unroll
    for (int w = 0; w < 7; ++w) sv[w] = stp[w];
    float mean = 0.f, M2 = 0.f;
#pragma unroll
    for (int w = 0; w < 7; ++w) { mean += sv[w][0]; M2 += sv[w][1]; }
    mean *= (1.f / 7.f);
    float dm = 0.f;
#pragma unroll
    for (int w = 0; w < 7; ++w) { const float d = sv[w][0] - mean; dm = fmaf(d, d, dm); }
    mbs[rep] = rbf(mean); rds[rep] = ln_rden(fmaf(64.f, dm, M2) * (1.f / DM));
    hv = rbf(rbf(hv - mbs[rep]) * rds[rep]);
  }
  float g1 = hs[14 * jo + 2 * (lane < 7 ? lane : 6) + par];
  g1 = rbf(rbf(g1 - mbs[0]) * rds[0]);
  g1 = rbf(rbf(g1 - mbs[1]) * rds[1]);
  const float g2 = g1 * g1, g3 = g2 * g1;
  STAMP(13);
  float ma[7];
#pragma unroll
  for (int mo = 0; mo < 7; ++mo) {
    const float h1 = __builtin_bit_cast(float, __builtin_amdgcn_readlane(__builtin_bit_cast(int, g1), mo));
    const float h2 = __builtin_bit_cast(float, __builtin_amdgcn_readlane(__builtin_bit_cast(int, g2), mo));
    const float h3 = __builtin_bit_cast(float, __builtin_amdgcn_readlane(__builtin_bit_cast(int, g3), mo));
    const f32x2 H1 = {h1, h1}, H2 = {h2, h2}, H3 = {h3, h3};
    f32x2 d[4], a[4];
#pragma unroll
    for (int i = 0; i < 4; ++i) {
      const f32x2 z = __builtin_elementwise_fma(c3[i], H3, __builtin_elementwise_fma(c2c[i], H2, __builtin_elementwise_fma(c1[i], H1, c0[i])));
      a[i] = __builtin_elementwise_fma(a1c[i], H1, a0c[i]);
      d[i][0] = __builtin_amdgcn_exp2f(z[0]);
      d[i][1] = i < 3 ? __builtin_amdgcn_exp2f(z[1]) : 0.f;
      d[i] += 1.f;
    }
    float acc1 = a[3][0] * __builtin_amdgcn_rcpf(d[3][0]);
    if (__any(fmaf(fabsf(h1), wmax, bmax) > 7.5f)) {
#pragma unroll
      for (int i = 0; i < 3; ++i) { acc1 = fmaf(a[i][0], __builtin_amdgcn_rcpf(d[i][0]), acc1); acc1 = fmaf(a[i][1], __builtin_amdgcn_rcpf(d[i][1]), acc1); }
    } else {
#pragma unroll
      for (int i = 0; i < 3; ++i) acc1 = fmaf(fmaf(a[i][0], d[i][1], a[i][1] * d[i][0]), __builtin_amdgcn_rcpf(d[i][0] * d[i][1]), acc1);
    }
    ma[mo] = acc1;
  }
  STAMP(14);
  {
    float x[8] = {ma[0], ma[1], ma[2], ma[3], ma[4], ma[5], ma[6], 0.f};
#define TR_STEP(HALF, CTRL, BIT) _Pragma("unroll") for (int i = 0; i < (HALF); ++i) { \
      const float lo_ = x[i], hi_ = x[i + (HALF)]; const float t1_ = lo_ + dpp<CTRL>(lo_), t2_ = hi_ + dpp<CTRL>(hi_); \
      x[i] = (c & (BIT)) ? t2_ : t1_; }
    TR_STEP(4, XOR8, 8) TR_STEP(2, XOR7, 4) TR_STEP(1, XOR2, 2)
#undef TR_STEP
    float t = x[0] + dpp<XOR1>(x[0]);
    t = x32_sum(x16_sum(t));
    float* red3 = reinterpret_cast<float*>(smem + L_RED3);
    red3[wave * 8 + (c >> 1)] = t;
    __syncthreads();
    if (tid < 14) {
      const int mo = tid >> 1, pr = tid & 1;
      const float y = (red3[(pr * 4 + 0) * 8 + mo] + red3[(pr * 4 + 1) * 8 + mo]) + (red3[(pr * 4 + 2) * 8 + mo] + red3[(pr * 4 + 3) * 8 + mo]);
      const int d = 14 * jo + tid;
      const float y2 = rbf(rbf(y) + lo2f(*reinterpret_cast<const unsigned*>(smem + L_EP + 8)));
      prm.out[b * DM + d] = (u16)bfbits(hs[d] + y2);
    }
  }
  STAMP(15);
}
}

extern "C" void kernel_launch(void* const* d_in, const int* in_sizes, int n_in, void* d_out, int out_size, void* d_ws, size_t ws_size,
                              hipStream_t stream) {
  Params p;
  p.x = (const u16*)d_in[0]; p.kc = (const u16*)d_in[1]; p.vc = (const u16*)d_in[2]; p.wq = (const u16*)d_in[3]; p.wo = (const u16*)d_in[4];
  p.w1 = (const u16*)d_in[6]; p.b1 = (const u16*)d_in[7]; p.w2 = (const u16*)d_in[8]; p.b2 = (const u16*)d_in[9];
  p.out = (u16*)d_out; p.ws = (unsigned char*)d_ws;
  k_fused<<<NWG, NT, 0, stream>>>(p);
}
```

```cpp
#include <hip/hip_runtime.h>
#include <hip/hip_bf16.h>
#include <stdint.h>
#include <type_traits>

#define STAMPS 0

namespace {
constexpr int NB = 8, DM = 448, HQ = 16, HKV = 8, SK = 8192, DH = 256, FF = 1792;
constexpr int NWG = 256, NT = 512, NWV = 8;
constexpr int NSPLIT = 4;
constexpr int TK = 16;
constexpr int NTILE = SK / TK / (NSPLIT * NWV);

typedef unsigned u32x4 __attribute__((ext_vector_type(4)));
typedef float f32x4 __attribute__((ext_vector_type(4)));
typedef float f32x2 __attribute__((ext_vector_type(2)));
typedef __bf16 bf16x2 __attribute__((ext_vector_type(2)));
typedef __bf16 bf16x8 __attribute__((ext_vector_type(8)));
typedef short s16x4 __attribute__((ext_vector_type(4)));
typedef short s16x8 __attribute__((ext_vector_type(8)));
typedef unsigned short u16;
typedef unsigned long long u64;
#define LAS __attribute__((address_space(3)))
#define GAS __attribute__((address_space(1)))
typedef GAS u64 gu64;
#define RLX_AGENT __ATOMIC_RELAXED, __HIP_MEMORY_SCOPE_AGENT

__device__ __forceinline__ float rbf(float x) { return (float)(__bf16)x; }
__device__ __forceinline__ unsigned bfbits(float x) { return (unsigned)__builtin_bit_cast(u16, (__bf16)x); }
__device__ __forceinline__ float lo2f(unsigned u) { return __builtin_bit_cast(float, u << 16); }
__device__ __forceinline__ float hi2f(unsigned u) { return __builtin_bit_cast(float, u & 0xffff0000u); }
__device__ __forceinline__ float dot2(unsigned k, unsigned q, float acc) {
  return __builtin_amdgcn_fdot2_f32_bf16(__builtin_bit_cast(bf16x2, k), __builtin_bit_cast(bf16x2, q), acc, false);
}
template <int CTRL> __device__ __forceinline__ float dpp(float x) {
  return __builtin_bit_cast(float, __builtin_amdgcn_mov_dpp(__builtin_bit_cast(int, x), CTRL, 0xf, 0xf, true));
}
constexpr int XOR1 = 0xB1, XOR2 = 0x4E, XOR7 = 0x141, XOR8 = 0x128, ROR4 = 0x124;
__device__ __forceinline__ float x16_sum(float x) {
  auto s = __builtin_amdgcn_permlane16_swap(__float_as_uint(x), __float_as_uint(x), false, false);
  return __uint_as_float(s[0]) + __uint_as_float(s[1]);
}
__device__ __forceinline__ float x32_sum(float x) {
  auto t = __builtin_amdgcn_permlane32_swap(__float_as_uint(x), __float_as_uint(x), false, false);
  return __uint_as_float(t[0]) + __uint_as_float(t[1]);
}
__device__ __forceinline__ float x16_max(float x) {
  auto s = __builtin_amdgcn_permlane16_swap(__float_as_uint(x), __float_as_uint(x), false, false);
  return fmaxf(__uint_as_float(s[0]), __uint_as_float(s[1]));
}
__device__ __forceinline__ float x32_max(float x) {
  auto t = __builtin_amdgcn_permlane32_swap(__float_as_uint(x), __float_as_uint(x), false, false);
  return fmaxf(__uint_as_float(t[0]), __uint_as_float(t[1]));
}
__device__ __forceinline__ float wave_sum(float x) {
  x += dpp<XOR1>(x); x += dpp<XOR2>(x); x += dpp<XOR7>(x); x += dpp<XOR8>(x);
  return x32_sum(x16_sum(x));
}
__device__ __forceinline__ void wave_sum2(float& a, float& b) {
#define WS2(E) { const float ta = E(a), tb = E(b); a = ta; b = tb; asm volatile("" : "+v"(a), "+v"(b)); }
  auto s1 = [](float x) { return x + dpp<XOR1>(x); }; auto s2 = [](float x) { return x + dpp<XOR2>(x); };
  auto s3 = [](float x) { return x + dpp<XOR7>(x); }; auto s4 = [](float x) { return x + dpp<XOR8>(x); };
  WS2(s1) WS2(s2) WS2(s3) WS2(s4)
#undef WS2
  { const auto t = __builtin_amdgcn_permlane16_swap(__float_as_uint(a), __float_as_uint(b), false, false);
    float u = __uint_as_float(t[0]) + __uint_as_float(t[1]);
    u = x32_sum(u);
    a = __builtin_bit_cast(float, __builtin_amdgcn_readlane(__builtin_bit_cast(int, u), 0));
    b = __builtin_bit_cast(float, __builtin_amdgcn_readlane(__builtin_bit_cast(int, u), 16)); }
}
__device__ __forceinline__ u32x4 ldnt(const void* p) { return __builtin_nontemporal_load(reinterpret_cast<const u32x4*>(p)); }
__device__ __forceinline__ u64 gload(const unsigned char* p) { return __hip_atomic_load((gu64*)p, RLX_AGENT); }
__device__ __forceinline__ void gstore(unsigned char* p, u64 v) { __hip_atomic_store((gu64*)p, v, RLX_AGENT); }
#define SB() __builtin_amdgcn_sched_barrier(0)

constexpr size_t WS_EPOCH = 0;
constexpr size_t WS_READY = 4096;
constexpr size_t WS_Q = 8192;
constexpr int PREC = 576;
constexpr size_t WS_P = WS_Q + (size_t)64 * 4 * 512 * 8;
constexpr size_t WS_Y = WS_P + (size_t)NWG * PREC * 4;
constexpr size_t WS_END = WS_Y + (size_t)NB * 8 * DM * 4;
constexpr size_t WS_STAMP = (size_t)16 << 20;
constexpr unsigned SPIN_CAP = 1u << 18;
constexpr unsigned EMPTY = 0xffffffffu;
constexpr int NTW = SK / TK / NSPLIT;

constexpr int L_WSL = 0;
constexpr int L_RED = L_WSL + 8 * 2048;
constexpr int L_QB = L_RED + 4 * 4096;
constexpr int L_PL = L_QB + 1024;
constexpr int L_MRG = L_PL + 1024;
constexpr int L_MW = L_MRG + NWV * 2 * 260 * 4;
constexpr int L_XR = L_MW + 3 * FF * 2;
constexpr int L_EP = L_XR + 896;
constexpr int L_ST = L_EP + 16;
constexpr int L_QF = L_ST + 64;
constexpr int L_DUMMY = L_QF + 16;
constexpr int L_T0 = L_DUMMY + 16;
constexpr int T_OB = 0;
constexpr int T_RED2 = T_OB + 2048;
constexpr int T_HS = T_RED2 + 8192;
constexpr int T_RED3 = T_HS + 2048;
constexpr int T_ST = T_RED3 + 256;
constexpr int T_END = T_ST + 128;
constexpr int L_END = L_T0 + T_END;
static_assert(T_END <= L_QB, "warm-up scratch fits the prologue area");
constexpr unsigned OOB = 0x40000000u;
#define EXP 0
#define TOUCH 0
#define WARM 0
#define PACE 12
#define POLL_DELAY 8
#define SPIN_SLEEP 2
#define RDY_AT 80

struct Params {
  const u16* x; const u16* kc; const u16* vc; const u16* wq; const u16* wo;
  const u16* w1; const u16* b1; const u16* w2; const u16* b2;
  u16* out; unsigned char* ws;
};

#define STAMP(i) do {} while (0)
typedef unsigned u32x2 __attribute__((ext_vector_type(2)));
__device__ __forceinline__ unsigned pubbits(float v) { return min(__float_as_uint(v), 0xfffffffeu); }
__device__ __forceinline__ bool full4(u32x4 v) { return max(max(v[0], v[1]), max(v[2], v[3])) != EMPTY; }
__device__ __forceinline__ float ln_rden(float var) { return __builtin_amdgcn_rcpf(rbf(__builtin_amdgcn_sqrtf(rbf(rbf(var) + 1e-5f)))); }
template <int N> using IC = std::integral_constant<int, N>;

__global__ void __launch_bounds__(NT, 2) k_fused(Params prm) {
  __shared__ __attribute__((aligned(16))) unsigned char smem[L_END];
  unsigned char* const ws = prm.ws;
  const int tid = threadIdx.x, lane = tid & 63, wave = __builtin_amdgcn_readfirstlane(tid >> 6);
  const int bid = blockIdx.x, kvh = bid & 7, b = (bid >> 3) & 7, s = bid >> 6, jq = bid >> 3;
  const int r = lane >> 4, c = lane & 15, r2 = lane >> 5, c2 = lane & 31;
  STAMP(0);
  const __amdgpu_buffer_rsrc_t wsr = __builtin_amdgcn_make_buffer_rsrc(ws, 0, (int)WS_END, 0x00020000);
  constexpr int SC1 = 16;
  const unsigned prec = (unsigned)WS_P + (unsigned)bid * PREC * 4;
  const int bg = jq / 7, cq = jq - bg * 7;
  const unsigned yout = (unsigned)WS_Y + (unsigned)(((2 * bg) * 8 + kvh) * DM + cq * 64) * 4;
  const int rg = jq >> 3, cg = jq & 7;
  u32x4 xv[2], wv[2][2]; u32x2 xa[2]; u64 ep0;
  const unsigned qout = (unsigned)WS_Q + (unsigned)(((kvh * 8 + 2 * (wave & 3) + r2) * 4 + rg) * 512 + cg * 64 + 2 * c2) * 8;
  u32x4 touch;
  if (wave < 4 && !(EXP & 2)) {
#pragma unroll
    for (int e = 0; e < 2; ++e) {
      const int ks = (wave + 4 * e) < 7 ? wave + 4 * e : 6;
      xv[e] = ldnt(prm.x + (wave + 4 * e) * DM + (lane < 56 ? lane : 55) * 8);
      xa[e] = __builtin_nontemporal_load(reinterpret_cast<const u32x2*>(prm.x + (c & 7) * DM + rg * 112 + 16 * ks + 4 * r));
      const u16* wqp = prm.wq + (size_t)(rg * 112 + 16 * ks + (lane >> 3)) * 4096 + kvh * 512 + cg * 64 + (lane & 7) * 8;
      wv[e][0] = ldnt(wqp);
      wv[e][1] = ldnt(wqp + 8 * 4096);
    }
    ep0 = gload(ws + WS_EPOCH);
  }
  *reinterpret_cast<u32x2*>(smem + L_QF) = u32x2{0u, 0u};
  SB();
  __syncthreads();
  SB();
  u64 epoch = 0;
  LAS unsigned* const qf = (LAS unsigned*)(smem + L_QF);
  if (wave < 4 && !(EXP & 2)) {
    auto qsync = [&](unsigned target) {
      asm volatile("s_waitcnt lgkmcnt(0)" ::: "memory");
      if (lane == 0) __hip_atomic_fetch_add(qf + 1, 1u, __ATOMIC_RELAXED, __HIP_MEMORY_SCOPE_WORKGROUP);
      while (__hip_atomic_load(qf + 1, __ATOMIC_RELAXED, __HIP_MEMORY_SCOPE_WORKGROUP) < target) {}
      asm volatile("" ::: "memory");
    };
    u16* wsl = reinterpret_cast<u16*>(smem + L_WSL) + wave * 2048;
    float* red = reinterpret_cast<float*>(smem + L_RED);
    STAMP(1);
#pragma unroll
    for (int e = 0; e < 2; ++e) {
      *reinterpret_cast<u32x4*>(wsl + e * 1024 + (lane >> 3) * 64 + (lane & 7) * 8) = wv[e][0];
      *reinterpret_cast<u32x4*>(wsl + e * 1024 + (8 + (lane >> 3)) * 64 + (lane & 7) * 8) = wv[e][1];
    }
#pragma unroll
    for (int e = 0; e < 2; ++e) {
      float xf[8];
#pragma unroll
      for (int k = 0; k < 4; ++k) { xf[2 * k] = lo2f(xv[e][k]); xf[2 * k + 1] = hi2f(xv[e][k]); }
      float sm = 0.f;
#pragma unroll
      for (int k = 0; k < 8; ++k) sm += xf[k];
      if (lane >= 56) sm = 0.f;
      const float mean = wave_sum(sm) * (1.f / DM);
      float vs = 0.f;
#pragma unroll
      for (int k = 0; k < 8; ++k) { const float d = xf[k] - mean; vs += d * d; }
      if (lane >= 56) vs = 0.f;
      const float var = wave_sum(vs) * (1.f / DM);
      *reinterpret_cast<f32x2*>(smem + L_ST + (wave + 4 * e) * 8) = f32x2{rbf(mean), ln_rden(var)};
      *reinterpret_cast<u32x4*>(smem + ((wave + 4 * e == b && lane < 56) ? L_XR + lane * 16 : L_DUMMY)) = xv[e];
    }
    s16x4 bfr[2][4];
#pragma unroll
    for (int e = 0; e < 2; ++e)
#pragma unroll
      for (int ct = 0; ct < 4; ++ct) bfr[e][ct] = __builtin_amdgcn_ds_read_tr16_b64_v4i16((LAS s16x4*)(wsl + e * 1024 + (4 * r + (c >> 2)) * 64 + ct * 16 + (c & 3) * 4));
    asm volatile("" : "+v"(ep0));
    epoch = ep0 + 1ull;
    *reinterpret_cast<u64*>(smem + L_EP) = epoch;
    STAMP(2);
    qsync(4);
    {
      const f32x2 st = *reinterpret_cast<const f32x2*>(smem + L_ST + (c & 7) * 8);
      u32x2 av[2];
#pragma unroll
      for (int e = 0; e < 2; ++e)
#pragma unroll
        for (int k = 0; k < 2; ++k) av[e][k] = bfbits(rbf(lo2f(xa[e][k]) - st[0]) * st[1]) | (bfbits(rbf(hi2f(xa[e][k]) - st[0]) * st[1]) << 16);
      if (wave == 3) av[1] = u32x2{0u, 0u};
#pragma unroll
      for (int ct = 0; ct < 4; ++ct) {
        f32x4 d = __builtin_amdgcn_mfma_f32_16x16x16bf16_1k(__builtin_bit_cast(s16x4, av[0]), bfr[0][ct], f32x4{0.f, 0.f, 0.f, 0.f}, 0, 0, 0);
        d = __builtin_amdgcn_mfma_f32_16x16x16bf16_1k(__builtin_bit_cast(s16x4, av[1]), bfr[1][ct], d, 0, 0, 0);
        float* o = red + wave * 1024 + 4 * r * 64 + ct * 16 + c;
        o[0] = d[0]; o[64] = d[1]; o[128] = d[2]; o[192] = d[3];
      }
    }
    qsync(8);
    STAMP(3);
    {
      const int bq = 2 * wave + r2;
      const f32x2* rp = reinterpret_cast<const f32x2*>(red + bq * 64 + 2 * c2);
      const f32x2 S = (rp[0] + rp[512]) + (rp[1024] + rp[1536]);
      const unsigned tlo = (unsigned)(epoch << 24), thi = (unsigned)(epoch >> 8);
      const u32x4 gq = {tlo | ((__float_as_uint(S[0]) + 0x80u) >> 8), thi, tlo | ((__float_as_uint(S[1]) + 0x80u) >> 8), thi};
      __builtin_amdgcn_raw_buffer_store_b128(gq, wsr, qout, 0, SC1);
    }
    SB();
    qsync(12);
    STAMP(4);
    if (wave == 0) {
      const unsigned qin = (unsigned)WS_Q + (unsigned)(kvh * 8 + b) * (4 * 512 * 8);
      const unsigned wlo = (unsigned)(epoch << 24), whi = (unsigned)(epoch >> 8);
      u32x4 g[16];
      unsigned spins = 0;
      __builtin_amdgcn_s_sleep(POLL_DELAY);
      for (;;) {
        asm volatile("" ::: "memory");
#pragma unroll
        for (int i = 0; i < 16; ++i) g[i] = __builtin_amdgcn_raw_buffer_load_b128(wsr, lane * 16 + i * 1024, qin, SC1);
        unsigned bad = 0;
#pragma unroll
        for (int i = 0; i < 16; ++i) bad |= ((g[i][0] ^ wlo) & 0xff000000u) | (g[i][1] ^ whi) | ((g[i][2] ^ wlo) & 0xff000000u) | (g[i][3] ^ whi);
        if (__all(bad == 0)) break;
        __builtin_amdgcn_s_sleep(1);
        if (++spins > SPIN_CAP) break;
      }
#pragma unroll
      for (int j = 0; j < 4; ++j) {
        float q0 = 0.f, q1 = 0.f;
#pragma unroll
        for (int k4 = 0; k4 < 4; ++k4) { q0 += __uint_as_float(g[k4 * 4 + j][0] << 8); q1 += __uint_as_float(g[k4 * 4 + j][2] << 8); }
        *reinterpret_cast<unsigned*>(smem + L_QB + (j * 128 + 2 * lane) * 2) = bfbits(q0) | (bfbits(q1) << 16);
      }
      __hip_atomic_store(qf, 1u, __ATOMIC_RELEASE, __HIP_MEMORY_SCOPE_WORKGROUP);
      STAMP(4);
    }
  }
  const size_t pair_off = (size_t)(b * HKV + kvh) * SK * DH * 2 + (size_t)s * NWV * (TK * DH * 2);
  const unsigned char* const kbase = reinterpret_cast<const unsigned char*>(prm.kc) + pair_off;
  const unsigned char* const vbase = reinterpret_cast<const unsigned char*>(prm.vc) + pair_off;
  auto tile_off = [](int T) { return (size_t)(unsigned)(((T >> 3) * 32 + (T & 7)) * (TK * DH * 2)); };
  const unsigned koff = r * 512 + c * 16, voff = lane * 16;
  u32x4 K[8], V[8], KB[8], VB[8];
  {
#define PACE_WAIT() do { SB(); asm volatile("s_waitcnt vmcnt(%0)" :: "i"(PACE - 4) : "memory"); SB(); } while (0)
    auto prefill = [&](u32x4 (&Kr)[8], u32x4 (&Vr)[8], auto SET) {
      constexpr int set = decltype(SET)::value;
      const unsigned char* kp = kbase + tile_off(set * NWV + wave);
      const unsigned char* vp = vbase + tile_off(set * NWV + wave);
      if constexpr (set * 16 >= PACE) PACE_WAIT();
#pragma unroll
      for (int i = 0; i < 4; ++i) Kr[i] = ldnt(kp + (i >> 1) * 2048 + (i & 1) * 256 + koff);
      if constexpr (set * 16 + 4 >= PACE) PACE_WAIT();
#pragma unroll
      for (int i = 4; i < 8; ++i) Kr[i] = ldnt(kp + (i >> 1) * 2048 + (i & 1) * 256 + koff);
      if constexpr (set * 16 + 8 >= PACE) PACE_WAIT();
#pragma unroll
      for (int i = 0; i < 4; ++i) Vr[i] = ldnt(vp + i * 1024 + voff);
      if constexpr (set * 16 + 12 >= PACE) PACE_WAIT();
#pragma unroll
      for (int i = 4; i < 8; ++i) Vr[i] = ldnt(vp + i * 1024 + voff);
      SB();
    };
    prefill(K, V, IC<0>{});
    if (wave == 7) {
      const u32x4 e4 = {EMPTY, EMPTY, EMPTY, EMPTY};
      __builtin_amdgcn_raw_buffer_store_b128(e4, wsr, prec + lane * 16, 0, SC1);
      __builtin_amdgcn_raw_buffer_store_b128(e4, wsr, prec + 1024 + lane * 16, 0, SC1);
      const unsigned yo = yout + (lane >= 32 ? 8 * DM * 4 : 0) + (lane & 15) * 16;
      __builtin_amdgcn_raw_buffer_store_b128(e4, wsr, lane < 16 ? prec + 2048 + lane * 16 : ((lane < 48 && jq < 28) ? yo : OOB), 0, SC1);
    }
    SB();
    prefill(KB, VB, IC<1>{});
  }
  while (__hip_atomic_load(qf, __ATOMIC_ACQUIRE, __HIP_MEMORY_SCOPE_WORKGROUP) == 0u) __builtin_amdgcn_s_sleep(1);
  epoch = *reinterpret_cast<const u64*>(smem + L_EP);
  u32x4 q[2][2];
#pragma unroll
  for (int h = 0; h < 2; ++h)
#pragma unroll
    for (int j = 0; j < 2; ++j) q[h][j] = *reinterpret_cast<const u32x4*>(smem + L_QB + (h * 256 + (c + 16 * j) * 8) * 2);
  STAMP(5);

  float m = -INFINITY, l = 0.f;
  f32x2 acc[2][4];
#pragma unroll
  for (int h = 0; h < 2; ++h)
#pragma unroll
    for (int k = 0; k < 4; ++k) acc[h][k] = f32x2{0.f, 0.f};
  float* pl = reinterpret_cast<float*>(smem + L_PL) + wave * 32;
  const int pw = (r & 1) * 16 + ((c >> 2) * 2 + (r >> 1)) * 2 + ((c >> 1) & 1);
  constexpr float C2 = 0.0625f * 1.4426950408889634f;
  const u16* wp = prm.wo + (jq < 28 ? (size_t)(kvh * 512 + 64 * wave + 32 * ((lane >> 3) & 1) + 8 * r) * DM + cq * 64 : (size_t)0) + (lane & 7) * 8;

  auto tile = [&](u32x4 (&Kr)[8], u32x4 (&Vr)[8], auto MODE, int T) {
    constexpr int mode = decltype(MODE)::value;
    const unsigned char* kp = kbase + tile_off(T);
    const unsigned char* vp = vbase + tile_off(T);
    float x[8];
#pragma unroll
    for (int g = 0; g < 4; ++g) {
#pragma unroll
      for (int h = 0; h < 2; ++h) {
        float d = 0.f;
#pragma unroll
        for (int j = 0; j < 2; ++j)
#pragma unroll
          for (int k = 0; k < 4; ++k) d = dot2(Kr[2 * g + j][k], q[h][j][k], d);
        x[2 * g + h] = d;
      }
      if constexpr (mode == 0) {
        Kr[2 * g] = ldnt(kp + g * 2048 + koff);
        Kr[2 * g + 1] = ldnt(kp + g * 2048 + 256 + koff);
      } else if constexpr (mode == 2) {
        Kr[2 * g] = ldnt(wp + (2 * g) * DM);
        Kr[2 * g + 1] = ldnt(wp + (2 * g + 1) * DM);
      }
      SB();
    }
#define TR_STEP(HALF, CTRL, BIT) _Pragma("unroll") for (int i = 0; i < (HALF); ++i) { \
      const float lo_ = x[i], hi_ = x[i + (HALF)]; const float t1_ = lo_ + dpp<CTRL>(lo_), t2_ = hi_ + dpp<CTRL>(hi_); \
      x[i] = (c & (BIT)) ? t2_ : t1_; }
    TR_STEP(4, XOR8, 8) TR_STEP(2, XOR7, 4) TR_STEP(1, XOR2, 2)
#undef TR_STEP
    float sc = x[0] + dpp<XOR1>(x[0]);
    sc = rbf(sc) * C2;
    if (__any(sc > m)) {
      float pm = sc;
      pm = fmaxf(pm, dpp<ROR4>(pm)); pm = fmaxf(pm, dpp<XOR8>(pm));
      pm = x32_max(x16_max(pm));
      const float mn = fmaxf(m, pm);
      const float alpha = __builtin_amdgcn_exp2f(m - mn);
      m = mn; l *= alpha;
      const float a0 = dpp<0x00>(alpha), a1 = dpp<0xAA>(alpha);
#pragma unroll
      for (int k = 0; k < 4; ++k) { acc[0][k] *= a0; acc[1][k] *= a1; }
    }
    const float p = __builtin_amdgcn_exp2f(sc - m);
    l += p;
    pl[pw] = p;
    f32x4 ppv[4];
#pragma unroll
    for (int i2 = 0; i2 < 4; ++i2) ppv[i2] = *reinterpret_cast<const f32x4*>(pl + r2 * 16 + i2 * 4);
    SB();
#pragma unroll
    for (int i2 = 0; i2 < 4; ++i2) {
      const f32x4 pp = ppv[i2];
#pragma unroll
      for (int e = 0; e < 2; ++e) {
        const int i = 2 * i2 + e;
        const float p0 = pp[2 * e], p1 = pp[2 * e + 1];
#pragma unroll
        for (int k = 0; k < 4; ++k) {
          const f32x2 vv = {lo2f(Vr[i][k]), hi2f(Vr[i][k])};
          acc[0][k] = __builtin_elementwise_fma(f32x2{p0, p0}, vv, acc[0][k]);
          acc[1][k] = __builtin_elementwise_fma(f32x2{p1, p1}, vv, acc[1][k]);
        }
        asm volatile("" : "+v"(acc[0][0]), "+v"(acc[0][1]), "+v"(acc[0][2]), "+v"(acc[0][3]), "+v"(acc[1][0]), "+v"(acc[1][1]), "+v"(acc[1][2]), "+v"(acc[1][3]) :: "memory");
        if constexpr (mode == 0) Vr[i] = ldnt(vp + i * 1024 + voff);
        SB();
      }
    }
  };
  tile(K, V, IC<0>{}, 2 * NWV + wave);
  if (wave == 7) {
    asm volatile("s_waitcnt vmcnt(16)" ::: "memory");
    __builtin_amdgcn_raw_buffer_store_b64(__builtin_bit_cast(u32x2, epoch), wsr, lane == 0 ? (unsigned)WS_READY + bid * 8 : OOB, 0, SC1);
  }
  tile(KB, VB, IC<2>{}, 0);
  u32x4 mw[3];
  const int mi = tid >= 256 && tid < 480 ? tid - 256 : 0;
  mw[0] = ldnt(prm.w1 + mi * 8);
  mw[1] = ldnt(prm.b1 + mi * 8);
  mw[2] = ldnt(prm.w2 + mi * 8);
  const unsigned b2r = __builtin_nontemporal_load(prm.b2);
  SB();
  tile(K, V, IC<0>{}, 3 * NWV + wave);
  u32x4 BF[8];
#pragma unroll
  for (int p = 0; p < 4; ++p)
#pragma unroll
    for (int k = 0; k < 4; ++k) {
      BF[2 * k][p] = __builtin_amdgcn_perm(KB[2 * p + 1][k], KB[2 * p][k], 0x05040100u);
      BF[2 * k + 1][p] = __builtin_amdgcn_perm(KB[2 * p + 1][k], KB[2 * p][k], 0x07060302u);
    }
#pragma unroll
  for (int j = 0; j < 8; ++j) asm volatile("" : "+v"(BF[j]));
  *reinterpret_cast<unsigned*>(smem + L_EP + 8) = b2r;
#pragma unroll
  for (int i = 0; i < 3; ++i) *reinterpret_cast<u32x4*>(smem + L_MW + i * FF * 2 + mi * 16) = mw[i];
  SB();
  int nxt = 4 * NWV + wave;
  for (; nxt < RDY_AT; nxt += NWV) tile(K, V, IC<0>{}, nxt);
  const unsigned char* rdyp = ws + WS_READY + (wave * 32 + c2) * 8;
  u64 rdy = gload(rdyp);
  SB();
  for (; nxt < NTW; nxt += NWV) tile(K, V, IC<0>{}, nxt);
  tile(K, V, IC<1>{}, 0);
  STAMP(6);
  if (!__all(rdy == epoch)) {
    unsigned spins = 0;
    do {
      __builtin_amdgcn_s_sleep(1);
      rdy = gload(rdyp);
    } while (!__all(rdy == epoch) && ++spins < SPIN_CAP);
  }
  l += dpp<ROR4>(l); l += dpp<XOR8>(l); l = x32_sum(x16_sum(l));
  float* mrg = reinterpret_cast<float*>(smem + L_MRG);
  {
    float* rec = mrg + (wave * 2 + r2) * 260;
    float a8[8];
#pragma unroll
    for (int k = 0; k < 8; ++k) {
      const auto t = __builtin_amdgcn_permlane32_swap(__float_as_uint(acc[0][k >> 1][k & 1]), __float_as_uint(acc[1][k >> 1][k & 1]), false, false);
      a8[k] = __uint_as_float(t[0]) + __uint_as_float(t[1]);
    }
    *reinterpret_cast<f32x4*>(rec + 4 + c2 * 8) = f32x4{a8[0], a8[1], a8[2], a8[3]};
    *reinterpret_cast<f32x4*>(rec + 8 + c2 * 8) = f32x4{a8[4], a8[5], a8[6], a8[7]};
    *reinterpret_cast<f32x2*>(mrg + (wave * 2 + ((c >> 1) & 1)) * 260) = f32x2{m, l};
  }
  int pass = __builtin_amdgcn_readfirstlane((WARM && wave == 1) ? 0 : 1);
#pragma nounroll
  for (; pass < 2; ++pass) {
  const bool real = pass != 0;
  unsigned char* const tl = smem + (real ? L_T0 : 0);
  const unsigned oob = real ? 0u : OOB;
  if (real) __syncthreads();
  STAMP(7);
  if (wave < 2) {
    f32x2 ml[NWV];
    f32x4 pa[NWV];
#pragma unroll
    for (int w = 0; w < NWV; ++w) { ml[w] = *reinterpret_cast<const f32x2*>(mrg + (w * 2 + wave) * 260); pa[w] = *reinterpret_cast<const f32x4*>(mrg + (w * 2 + wave) * 260 + 4 + lane * 4); }
    float M = ml[0][0];
#pragma unroll
    for (int w = 1; w < NWV; ++w) M = fmaxf(M, ml[w][0]);
    float L = 0.f;
    f32x4 on = {0.f, 0.f, 0.f, 0.f};
#pragma unroll
    for (int w = 0; w < NWV; ++w) {
      const float wt = __builtin_amdgcn_exp2f(ml[w][0] - M);
      L = fmaf(wt, ml[w][1], L);
      on += wt * pa[w];
    }
    on *= __builtin_amdgcn_rcpf(L);
    const float lse = M + __builtin_amdgcn_logf(L);
    const u32x4 ob = {pubbits(on[0]), pubbits(on[1]), pubbits(on[2]), pubbits(on[3])};
    __builtin_amdgcn_raw_buffer_store_b128(ob, wsr, prec + tid * 16 + oob, 0, SC1);
    __builtin_amdgcn_raw_buffer_store_b32(pubbits(lse), wsr, lane == 0 ? prec + 2048 + wave * 128 + oob : OOB, 0, SC1);
  }
  if (bid == 0 && tid == 448) gstore(ws + WS_EPOCH, epoch);
  STAMP(8);
  constexpr float KC = 0.7978845608028654f, L2E = 1.4426950408889634f;
  constexpr float K1 = -2.f * KC * L2E, K2 = -2.f * KC * 0.044715f * L2E;
  f32x2 c0[4], c1[4], c2c[4], c3[4], a0c[4], a1c[4];
  float wmax = 0.f, bmax = 0.f;
  {
    const u16* mwp = reinterpret_cast<const u16*>(smem + L_MW);
#pragma unroll
    for (int i = 0; i < 7; ++i) {
      const int f = (tid & 255) + 256 * i;
      const float w = lo2f(mwp[f]), bb = lo2f(mwp[FF + f]), v = lo2f(mwp[2 * FF + f]);
      c3[i >> 1][i & 1] = K2 * w * w * w; c2c[i >> 1][i & 1] = 3.f * K2 * w * w * bb;
      c1[i >> 1][i & 1] = K1 * w + 3.f * K2 * w * bb * bb; c0[i >> 1][i & 1] = K1 * bb + K2 * bb * bb * bb;
      a1c[i >> 1][i & 1] = w * v; a0c[i >> 1][i & 1] = bb * v;
      wmax = fmaxf(wmax, fabsf(w)); bmax = fmaxf(bmax, fabsf(bb));
    }
    c0[3][1] = c1[3][1] = c2c[3][1] = c3[3][1] = a0c[3][1] = a1c[3][1] = 0.f;
#pragma unroll
    for (int i = 0; i < 4; ++i) asm volatile("" : "+v"(c0[i]), "+v"(c1[i]), "+v"(c2c[i]), "+v"(c3[i]), "+v"(a0c[i]), "+v"(a1c[i]));
    asm volatile("" : "+v"(wmax), "+v"(bmax));
  }
  if (jq < 28) {
    if (wave >= 4 || !real) {
      const int beta = (wave >> 1) & 1, h = wave & 1;
      u32x4 sa[4]; unsigned sl[4];
      unsigned spins = 0;
      for (;;) {
        asm volatile("" ::: "memory");
#pragma unroll
        for (int t = 0; t < 4; ++t) {
          const unsigned rec = (unsigned)WS_P + (unsigned)(t * 64 + (2 * bg + beta) * 8 + kvh) * PREC * 4;
          sa[t] = __builtin_amdgcn_raw_buffer_load_b128(wsr, h * 1024 + lane * 16 + oob, rec, SC1);
          sl[t] = __builtin_amdgcn_raw_buffer_load_b32(wsr, 2048 + h * 128 + oob, rec, SC1);
        }
        bool ok = true;
#pragma unroll
        for (int t = 0; t < 4; ++t) ok &= full4(sa[t]) && sl[t] != EMPTY;
        if (__all(ok)) break;
        __builtin_amdgcn_s_sleep(SPIN_SLEEP);
        if (++spins > SPIN_CAP) break;
      }
      const float l0 = __uint_as_float(sl[0]), l1 = __uint_as_float(sl[1]), l2 = __uint_as_float(sl[2]), l3 = __uint_as_float(sl[3]);
      const float MM = fmaxf(fmaxf(l0, l1), fmaxf(l2, l3));
      const float e0 = __builtin_amdgcn_exp2f(l0 - MM), e1 = __builtin_amdgcn_exp2f(l1 - MM), e2 = __builtin_amdgcn_exp2f(l2 - MM), e3 = __builtin_amdgcn_exp2f(l3 - MM);
      const float inv = __builtin_amdgcn_rcpf((e0 + e1) + (e2 + e3));
      const f32x4 oo = (e0 * __builtin_bit_cast(f32x4, sa[0]) + e1 * __builtin_bit_cast(f32x4, sa[1]) + e2 * __builtin_bit_cast(f32x4, sa[2]) + e3 * __builtin_bit_cast(f32x4, sa[3])) * inv;
      *reinterpret_cast<u32x2*>(tl + T_OB + (tid & 255) * 8) = u32x2{bfbits(oo[0]) | (bfbits(oo[1]) << 16), bfbits(oo[2]) | (bfbits(oo[3]) << 16)};
    }
    STAMP(9);
    if (real) __syncthreads();
    float* red = reinterpret_cast<float*>(tl + T_RED2);
    {
      const bf16x8 a = *reinterpret_cast<const bf16x8*>(tl + T_OB + (((c >> 1) & 1) * 512 + 64 * wave + 32 * (c & 1) + 8 * r) * 2);
      f32x4 y0[2], y1[2];
#pragma unroll
      for (int j = 0; j < 8; ++j) {
        const f32x4 d = __builtin_amdgcn_mfma_f32_16x16x32_bf16(a, __builtin_bit_cast(bf16x8, BF[j]), f32x4{0.f, 0.f, 0.f, 0.f}, 0, 0, 0);
        y0[j >> 2][j & 3] = (c & 8) ? d[1] : d[0];
        y1[j >> 2][j & 3] = (c & 8) ? d[3] : d[2];
      }
      float* o = red + (wave * 2 + (c >> 3)) * 128 + (c & 7) * 8;
      *reinterpret_cast<f32x4*>(o) = y0[0]; *reinterpret_cast<f32x4*>(o + 4) = y0[1];
      *reinterpret_cast<f32x4*>(o + 64) = y1[0]; *reinterpret_cast<f32x4*>(o + 68) = y1[1];
    }
    STAMP(10);
    if (real) __syncthreads();
    if (wave == 0 || !real) {
      f32x4 t = *reinterpret_cast<const f32x4*>(red + (r2 * 8) * 128 + c2 * 4);
#pragma unroll
      for (int w = 1; w < 8; ++w) t += *reinterpret_cast<const f32x4*>(red + (r2 * 8 + w) * 128 + c2 * 4);
      const u32x4 yb = {pubbits(x32_sum(t[0])), pubbits(x32_sum(t[1])), pubbits(x32_sum(t[2])), pubbits(x32_sum(t[3]))};
      __builtin_amdgcn_raw_buffer_store_b128(yb, wsr, lane < 32 ? yout + (lane >> 4) * (8 * DM * 4) + c * 16 + oob : OOB, 0, SC1);
    }
  }
  STAMP(11);
  float* hs = reinterpret_cast<float*>(tl + T_HS);
  float hv = 0.f;
  if (wave < 7) {
    const float xres = lo2f(reinterpret_cast<const u16*>(smem + L_XR)[tid]);
    const unsigned yin = (unsigned)WS_Y + (unsigned)b * (8 * DM * 4);
    unsigned yv[8];
    unsigned spins = 0;
    for (;;) {
      asm volatile("" ::: "memory");
#pragma unroll
      for (int k = 0; k < 8; ++k) yv[k] = __builtin_amdgcn_raw_buffer_load_b32(wsr, tid * 4 + k * (DM * 4) + oob, yin, SC1);
      unsigned mx = yv[0];
#pragma unroll
      for (int k = 1; k < 8; ++k) mx = max(mx, yv[k]);
      if (__all(mx != EMPTY)) break;
      __builtin_amdgcn_s_sleep(SPIN_SLEEP);
      if (++spins > SPIN_CAP) break;
    }
    const float t = ((__uint_as_float(yv[0]) + __uint_as_float(yv[1])) + (__uint_as_float(yv[2]) + __uint_as_float(yv[3]))) +
                    ((__uint_as_float(yv[4]) + __uint_as_float(yv[5])) + (__uint_as_float(yv[6]) + __uint_as_float(yv[7])));
    hv = rbf(xres + rbf(t));
    hs[tid] = hv;
  }
  STAMP(12);
  const int jo = kvh * 4 + s, par = wave >> 2;
  float mbs[2], rds[2];
  {
    const float cw = __builtin_bit_cast(float, __builtin_amdgcn_readfirstlane(__builtin_bit_cast(int, hv)));
    const float dv = hv - cw;
    float s1 = dv, s2 = dv * dv;
    wave_sum2(s1, s2);
    f32x2* stp = reinterpret_cast<f32x2*>(tl + T_ST);
    stp[wave] = f32x2{fmaf(s1, 1.f / 64.f, cw), fmaf(-s1 * (1.f / 64.f), s1, s2)};
    if (real) __syncthreads();
    f32x2 sv[7];
#pragma unroll
    for (int w = 0; w < 7; ++w) sv[w] = stp[w];
    const f32x2 tot = ((sv[0] + sv[1]) + (sv[2] + sv[3])) + ((sv[4] + sv[5]) + sv[6]);
    const float mean = tot[0] * (1.f / 7.f);
    float dm0 = 0.f, dm1 = 0.f;
#pragma unroll
    for (int w = 0; w < 7; ++w) { const float d = sv[w][0] - mean; if (w & 1) dm1 = fmaf(d, d, dm1); else dm0 = fmaf(d, d, dm0); }
    mbs[0] = rbf(mean); rds[0] = ln_rden(fmaf(64.f, dm0 + dm1, tot[1]) * (1.f / DM));
    hv = wave < 7 ? rbf(rbf(hv - mbs[0]) * rds[0]) : 0.f;
  }
  {
    float s1 = hv, s2 = hv * hv;
    wave_sum2(s1, s2);
    f32x2* stp = reinterpret_cast<f32x2*>(tl + T_ST + 64);
    stp[wave] = f32x2{s1, s2};
    if (real) __syncthreads();
    f32x2 sv[7];
#pragma unroll
    for (int w = 0; w < 7; ++w) sv[w] = stp[w];
    const f32x2 tot = (((sv[0] + sv[1]) + (sv[2] + sv[3])) + ((sv[4] + sv[5]) + sv[6])) * (1.f / DM);
    mbs[1] = rbf(tot[0]); rds[1] = ln_rden(fmaxf(fmaf(-tot[0], tot[0], tot[1]), 0.f));
  }
  float g1 = hs[14 * jo + 2 * (lane < 7 ? lane : 6) + par];
  g1 = rbf(rbf(g1 - mbs[0]) * rds[0]);
  g1 = rbf(rbf(g1 - mbs[1]) * rds[1]);
  if (!real) g1 = 0.f;
  const float g2 = g1 * g1, g3 = g2 * g1;
  STAMP(13);
  float ma[7];
  auto mlp = [&](auto SAFE) {
    constexpr bool safe = decltype(SAFE)::value != 0;
#pragma unroll
    for (int mo = 0; mo < 7; ++mo) {
      const float h1 = __builtin_bit_cast(float, __builtin_amdgcn_readlane(__builtin_bit_cast(int, g1), mo));
      const float h2 = __builtin_bit_cast(float, __builtin_amdgcn_readlane(__builtin_bit_cast(int, g2), mo));
      const float h3 = __builtin_bit_cast(float, __builtin_amdgcn_readlane(__builtin_bit_cast(int, g3), mo));
      const f32x2 H1 = {h1, h1}, H2 = {h2, h2}, H3 = {h3, h3};
      f32x2 d[4], a[4];
#pragma unroll
      for (int i = 0; i < 4; ++i) {
        const f32x2 z = __builtin_elementwise_fma(c3[i], H3, __builtin_elementwise_fma(c2c[i], H2, __builtin_elementwise_fma(c1[i], H1, c0[i])));
        a[i] = __builtin_elementwise_fma(a1c[i], H1, a0c[i]);
        d[i][0] = __builtin_amdgcn_exp2f(z[0]);
        d[i][1] = i < 3 ? __builtin_amdgcn_exp2f(z[1]) : 0.f;
        d[i] += 1.f;
      }
      float acc1 = a[3][0] * __builtin_amdgcn_rcpf(d[3][0]);
#pragma unroll
      for (int i = 0; i < 3; ++i) {
        if constexpr (safe) { acc1 = fmaf(a[i][0], __builtin_amdgcn_rcpf(d[i][0]), acc1); acc1 = fmaf(a[i][1], __builtin_amdgcn_rcpf(d[i][1]), acc1); }
        else acc1 = fmaf(fmaf(a[i][0], d[i][1], a[i][1] * d[i][0]), __builtin_amdgcn_rcpf(d[i][0] * d[i][1]), acc1);
      }
      ma[mo] = acc1;
    }
  };
  {
    float hm = fabsf(g1);
    hm = fmaxf(hm, dpp<XOR1>(hm)); hm = fmaxf(hm, dpp<XOR2>(hm)); hm = fmaxf(hm, dpp<XOR7>(hm));
    hm = __builtin_bit_cast(float, __builtin_amdgcn_readfirstlane(__builtin_bit_cast(int, hm)));
    if (__all(fmaf(hm, wmax, bmax) <= 7.5f)) mlp(IC<0>{}); else mlp(IC<1>{});
  }
  STAMP(14);
  {
    float x[8] = {ma[0], ma[1], ma[2], ma[3], ma[4], ma[5], ma[6], 0.f};
#define TR_STEP(HALF, CTRL, BIT) _Pragma("unroll") for (int i = 0; i < (HALF); ++i) { \
      const float lo_ = x[i], hi_ = x[i + (HALF)]; const float t1_ = lo_ + dpp<CTRL>(lo_), t2_ = hi_ + dpp<CTRL>(hi_); \
      x[i] = (c & (BIT)) ? t2_ : t1_; }
    TR_STEP(4, XOR8, 8) TR_STEP(2, XOR7, 4) TR_STEP(1, XOR2, 2)
#undef TR_STEP
    float t = x[0] + dpp<XOR1>(x[0]);
    t = x32_sum(x16_sum(t));
    float* red3 = reinterpret_cast<float*>(tl + T_RED3);
    red3[wave * 8 + (c >> 1)] = t;
    if (real) __syncthreads();
    if (wave == 0 || !real) {
      const int to = lane < 14 ? lane : 13;
      const int mo = to >> 1, pr = to & 1;
      const float y = (red3[(pr * 4 + 0) * 8 + mo] + red3[(pr * 4 + 1) * 8 + mo]) + (red3[(pr * 4 + 2) * 8 + mo] + red3[(pr * 4 + 3) * 8 + mo]);
      const int d = 14 * jo + to;
      const float y2 = rbf(rbf(y) + lo2f(*reinterpret_cast<const unsigned*>(smem + L_EP + 8)));
      const u16 ov = (u16)bfbits(hs[d] + y2);
      if (real && lane < 14) prm.out[b * DM + d] = ov;
    }
  }
  }
  STAMP(15);
}
}

extern "C" void kernel_launch(void* const* d_in, const int* in_sizes, int n_in, void* d_out, int out_size, void* d_ws, size_t ws_size,
                              hipStream_t stream) {
  Params p;
  p.x = (const u16*)d_in[0]; p.kc = (const u16*)d_in[1]; p.vc = (const u16*)d_in[2]; p.wq = (const u16*)d_in[3]; p.wo = (const u16*)d_in[4];
  p.w1 = (const u16*)d_in[6]; p.b1 = (const u16*)d_in[7]; p.w2 = (const u16*)d_in[8]; p.b2 = (const u16*)d_in[9];
  p.out = (u16*)d_out; p.ws = (unsigned char*)d_ws;
  k_fused<<<NWG, NT, 0, stream>>>(p);
}
```

```cpp
#include <hip/hip_runtime.h>
#include <hip/hip_bf16.h>
#include <stdint.h>
#include <type_traits>

#define STAMPS 0

namespace {
constexpr int NB = 8, DM = 448, HQ = 16, HKV = 8, SK = 8192, DH = 256, FF = 1792;
constexpr int NWG = 256, NT = 512, NWV = 8;
constexpr int NSPLIT = 4;
constexpr int TK = 16;
constexpr int NTILE = SK / TK / (NSPLIT * NWV);

typedef unsigned u32x4 __attribute__((ext_vector_type(4)));
typedef float f32x4 __attribute__((ext_vector_type(4)));
typedef float f32x2 __attribute__((ext_vector_type(2)));
typedef __bf16 bf16x2 __attribute__((ext_vector_type(2)));
typedef __bf16 bf16x8 __attribute__((ext_vector_type(8)));
typedef short s16x4 __attribute__((ext_vector_type(4)));
typedef short s16x8 __attribute__((ext_vector_type(8)));
typedef unsigned short u16;
typedef unsigned long long u64;
#define LAS __attribute__((address_space(3)))
#define GAS __attribute__((address_space(1)))
typedef GAS u64 gu64;
#define RLX_AGENT __ATOMIC_RELAXED, __HIP_MEMORY_SCOPE_AGENT

__device__ __forceinline__ float rbf(float x) { return (float)(__bf16)x; }
__device__ __forceinline__ unsigned bfbits(float x) { return (unsigned)__builtin_bit_cast(u16, (__bf16)x); }
__device__ __forceinline__ float lo2f(unsigned u) { return __builtin_bit_cast(float, u << 16); }
__device__ __forceinline__ float hi2f(unsigned u) { return __builtin_bit_cast(float, u & 0xffff0000u); }
__device__ __forceinline__ float dot2(unsigned k, unsigned q, float acc) {
  return __builtin_amdgcn_fdot2_f32_bf16(__builtin_bit_cast(bf16x2, k), __builtin_bit_cast(bf16x2, q), acc, false);
}
template <int CTRL> __device__ __forceinline__ float dpp(float x) {
  return __builtin_bit_cast(float, __builtin_amdgcn_mov_dpp(__builtin_bit_cast(int, x), CTRL, 0xf, 0xf, true));
}
constexpr int XOR1 = 0xB1, XOR2 = 0x4E, XOR7 = 0x141, XOR8 = 0x128, ROR4 = 0x124;
__device__ __forceinline__ float x16_sum(float x) {
  auto s = __builtin_amdgcn_permlane16_swap(__float_as_uint(x), __float_as_uint(x), false, false);
  return __uint_as_float(s[0]) + __uint_as_float(s[1]);
}
__device__ __forceinline__ float x32_sum(float x) {
  auto t = __builtin_amdgcn_permlane32_swap(__float_as_uint(x), __float_as_uint(x), false, false);
  return __uint_as_float(t[0]) + __uint_as_float(t[1]);
}
__device__ __forceinline__ float x16_max(float x) {
  auto s = __builtin_amdgcn_permlane16_swap(__float_as_uint(x), __float_as_uint(x), false, false);
  return fmaxf(__uint_as_float(s[0]), __uint_as_float(s[1]));
}
__device__ __forceinline__ float x32_max(float x) {
  auto t = __builtin_amdgcn_permlane32_swap(__float_as_uint(x), __float_as_uint(x), false, false);
  return fmaxf(__uint_as_float(t[0]), __uint_as_float(t[1]));
}
__device__ __forceinline__ float wave_sum(float x) {
  x += dpp<XOR1>(x); x += dpp<XOR2>(x); x += dpp<XOR7>(x); x += dpp<XOR8>(x);
  return x32_sum(x16_sum(x));
}
__device__ __forceinline__ void wave_sum2(float& a, float& b) {
#define WS2(E) { const float ta = E(a), tb = E(b); a = ta; b = tb; asm volatile("" : "+v"(a), "+v"(b)); }
  auto s1 = [](float x) { return x + dpp<XOR1>(x); }; auto s2 = [](float x) { return x + dpp<XOR2>(x); };
  auto s3 = [](float x) { return x + dpp<XOR7>(x); }; auto s4 = [](float x) { return x + dpp<XOR8>(x); };
  WS2(s1) WS2(s2) WS2(s3) WS2(s4)
#undef WS2
  { const auto t = __builtin_amdgcn_permlane16_swap(__float_as_uint(a), __float_as_uint(b), false, false);
    float u = __uint_as_float(t[0]) + __uint_as_float(t[1]);
    u = x32_sum(u);
    a = __builtin_bit_cast(float, __builtin_amdgcn_readlane(__builtin_bit_cast(int, u), 0));
    b = __builtin_bit_cast(float, __builtin_amdgcn_readlane(__builtin_bit_cast(int, u), 16)); }
}
__device__ __forceinline__ u32x4 ldnt(const void* p) { return __builtin_nontemporal_load(reinterpret_cast<const u32x4*>(p)); }
__device__ __forceinline__ u64 gload(const unsigned char* p) { return __hip_atomic_load((gu64*)p, RLX_AGENT); }
__device__ __forceinline__ void gstore(unsigned char* p, u64 v) { __hip_atomic_store((gu64*)p, v, RLX_AGENT); }
#define SB() __builtin_amdgcn_sched_barrier(0)

constexpr size_t WS_EPOCH = 0;
constexpr size_t WS_READY = 4096;
constexpr size_t WS_Q = 8192;
constexpr int PREC = 576;
constexpr size_t WS_P = WS_Q + (size_t)64 * 4 * 512 * 8;
constexpr size_t WS_Y = WS_P + (size_t)NWG * PREC * 4;
constexpr size_t WS_END = WS_Y + (size_t)NB * 8 * DM * 4;
constexpr size_t WS_STAMP = (size_t)16 << 20;
constexpr unsigned SPIN_CAP = 1u << 18;
constexpr unsigned EMPTY = 0xffffffffu;
constexpr int NTW = SK / TK / NSPLIT;

constexpr int L_WSL = 0;
constexpr int L_RED = L_WSL + 8 * 2048;
constexpr int L_QB = L_RED + 4 * 4096;
constexpr int L_PL = L_QB + 1024;
constexpr int L_MRG = L_PL + 1024;
constexpr int L_MW = L_MRG + NWV * 2 * 260 * 4;
constexpr int L_XR = L_MW + 3 * FF * 2;
constexpr int L_EP = L_XR + 896;
constexpr int L_ST = L_EP + 16;
constexpr int L_QF = L_ST + 64;
constexpr int L_DUMMY = L_QF + 16;
constexpr int L_T0 = L_DUMMY + 16;
constexpr int T_OB = 0;
constexpr int T_RED2 = T_OB + 2048;
constexpr int T_HS = T_RED2 + 8192;
constexpr int T_RED3 = T_HS + 2048;
constexpr int T_ST = T_RED3 + 256;
constexpr int T_END = T_ST + 128;
constexpr int L_END = L_T0 + T_END;
constexpr unsigned OOB = 0x40000000u;
#define EXP 0
#define NSW 2
#define DRAIN 8
#define TOUCH 0
#define WARM 0
#define PACE 12
#define POLL_DELAY 8
#define SPIN_SLEEP 2
#define RDY_AT 80

struct Params {
  const u16* x; const u16* kc; const u16* vc; const u16* wq; const u16* wo;
  const u16* w1; const u16* b1; const u16* w2; const u16* b2;
  u16* out; unsigned char* ws;
};

#define STAMP(i) do {} while (0)
typedef unsigned u32x2 __attribute__((ext_vector_type(2)));
__device__ __forceinline__ unsigned pubbits(float v) { return min(__float_as_uint(v), 0xfffffffeu); }
__device__ __forceinline__ bool full4(u32x4 v) { return max(max(v[0], v[1]), max(v[2], v[3])) != EMPTY; }
__device__ __forceinline__ float ln_rden(float var) { return __builtin_amdgcn_rcpf(rbf(__builtin_amdgcn_sqrtf(rbf(rbf(var) + 1e-5f)))); }
template <int N> using IC = std::integral_constant<int, N>;

__global__ void __launch_bounds__(NT, 2) k_fused(Params prm) {
  __shared__ __attribute__((aligned(16))) unsigned char smem[L_END];
  unsigned char* const ws = prm.ws;
  const int tid = threadIdx.x, lane = tid & 63, wave = __builtin_amdgcn_readfirstlane(tid >> 6);
  const int bid = blockIdx.x, kvh = bid & 7, b = (bid >> 3) & 7, s = bid >> 6, jq = bid >> 3;
  const int r = lane >> 4, c = lane & 15, r2 = lane >> 5, c2 = lane & 31;
  STAMP(0);
  const __amdgpu_buffer_rsrc_t wsr = __builtin_amdgcn_make_buffer_rsrc(ws, 0, (int)WS_END, 0x00020000);
  constexpr int SC1 = 16;
  const unsigned prec = (unsigned)WS_P + (unsigned)bid * PREC * 4;
  const int bg = jq / 7, cq = jq - bg * 7;
  const unsigned yout = (unsigned)WS_Y + (unsigned)(((2 * bg) * 8 + kvh) * DM + cq * 64) * 4;
  const int rg = jq >> 3, cg = jq & 7;
  u32x4 xv[2], wv[2][2]; u32x2 xa[2]; u64 ep0;
  const unsigned qout = (unsigned)WS_Q + (unsigned)(((kvh * 8 + 2 * (wave & 3) + r2) * 4 + rg) * 512 + cg * 64 + 2 * c2) * 8;
  u32x4 touch;
  if (wave < 4 && !(EXP & 2)) {
#pragma unroll
    for (int e = 0; e < 2; ++e) {
      const int ks = (wave + 4 * e) < 7 ? wave + 4 * e : 6;
      xv[e] = ldnt(prm.x + (wave + 4 * e) * DM + (lane < 56 ? lane : 55) * 8);
      xa[e] = __builtin_nontemporal_load(reinterpret_cast<const u32x2*>(prm.x + (c & 7) * DM + rg * 112 + 16 * ks + 4 * r));
      const u16* wqp = prm.wq + (size_t)(rg * 112 + 16 * ks + (lane >> 3)) * 4096 + kvh * 512 + cg * 64 + (lane & 7) * 8;
      wv[e][0] = ldnt(wqp);
      wv[e][1] = ldnt(wqp + 8 * 4096);
    }
    ep0 = gload(ws + WS_EPOCH);
  }
  *reinterpret_cast<u32x2*>(smem + L_QF) = u32x2{0u, 0u};
  SB();
  __syncthreads();
  SB();
  u64 epoch = 0;
  LAS unsigned* const qf = (LAS unsigned*)(smem + L_QF);
  if (wave < 4 && !(EXP & 2)) {
    auto qsync = [&](unsigned target) {
      asm volatile("s_waitcnt lgkmcnt(0)" ::: "memory");
      if (lane == 0) __hip_atomic_fetch_add(qf + 1, 1u, __ATOMIC_RELAXED, __HIP_MEMORY_SCOPE_WORKGROUP);
      while (__hip_atomic_load(qf + 1, __ATOMIC_RELAXED, __HIP_MEMORY_SCOPE_WORKGROUP) < target) {}
      asm volatile("" ::: "memory");
    };
    u16* wsl = reinterpret_cast<u16*>(smem + L_WSL) + wave * 2048;
    float* red = reinterpret_cast<float*>(smem + L_RED);
    STAMP(1);
#pragma unroll
    for (int e = 0; e < 2; ++e) {
      *reinterpret_cast<u32x4*>(wsl + e * 1024 + (lane >> 3) * 64 + (lane & 7) * 8) = wv[e][0];
      *reinterpret_cast<u32x4*>(wsl + e * 1024 + (8 + (lane >> 3)) * 64 + (lane & 7) * 8) = wv[e][1];
    }
#pragma unroll
    for (int e = 0; e < 2; ++e) {
      float xf[8];
#pragma unroll
      for (int k = 0; k < 4; ++k) { xf[2 * k] = lo2f(xv[e][k]); xf[2 * k + 1] = hi2f(xv[e][k]); }
      float sm = 0.f;
#pragma unroll
      for (int k = 0; k < 8; ++k) sm += xf[k];
      if (lane >= 56) sm = 0.f;
      const float mean = wave_sum(sm) * (1.f / DM);
      float vs = 0.f;
#pragma unroll
      for (int k = 0; k < 8; ++k) { const float d = xf[k] - mean; vs += d * d; }
      if (lane >= 56) vs = 0.f;
      const float var = wave_sum(vs) * (1.f / DM);
      *reinterpret_cast<f32x2*>(smem + L_ST + (wave + 4 * e) * 8) = f32x2{rbf(mean), ln_rden(var)};
      *reinterpret_cast<u32x4*>(smem + ((wave + 4 * e == b && lane < 56) ? L_XR + lane * 16 : L_DUMMY)) = xv[e];
    }
    s16x4 bfr[2][4];
#pragma unroll
    for (int e = 0; e < 2; ++e)
#pragma unroll
      for (int ct = 0; ct < 4; ++ct) bfr[e][ct] = __builtin_amdgcn_ds_read_tr16_b64_v4i16((LAS s16x4*)(wsl + e * 1024 + (4 * r + (c >> 2)) * 64 + ct * 16 + (c & 3) * 4));
    asm volatile("" : "+v"(ep0));
    epoch = ep0 + 1ull;
    *reinterpret_cast<u64*>(smem + L_EP) = epoch;
    STAMP(2);
    qsync(4);
    {
      const f32x2 st = *reinterpret_cast<const f32x2*>(smem + L_ST + (c & 7) * 8);
      u32x2 av[2];
#pragma unroll
      for (int e = 0; e < 2; ++e)
#pragma unroll
        for (int k = 0; k < 2; ++k) av[e][k] = bfbits(rbf(lo2f(xa[e][k]) - st[0]) * st[1]) | (bfbits(rbf(hi2f(xa[e][k]) - st[0]) * st[1]) << 16);
      if (wave == 3) av[1] = u32x2{0u, 0u};
#pragma unroll
      for (int ct = 0; ct < 4; ++ct) {
        f32x4 d = __builtin_amdgcn_mfma_f32_16x16x16bf16_1k(__builtin_bit_cast(s16x4, av[0]), bfr[0][ct], f32x4{0.f, 0.f, 0.f, 0.f}, 0, 0, 0);
        d = __builtin_amdgcn_mfma_f32_16x16x16bf16_1k(__builtin_bit_cast(s16x4, av[1]), bfr[1][ct], d, 0, 0, 0);
        float* o = red + wave * 1024 + 4 * r * 64 + ct * 16 + c;
        o[0] = d[0]; o[64] = d[1]; o[128] = d[2]; o[192] = d[3];
      }
    }
    qsync(8);
    STAMP(3);
    {
      const int bq = 2 * wave + r2;
      const f32x2* rp = reinterpret_cast<const f32x2*>(red + bq * 64 + 2 * c2);
      const f32x2 S = (rp[0] + rp[512]) + (rp[1024] + rp[1536]);
      const unsigned tlo = (unsigned)(epoch << 24), thi = (unsigned)(epoch >> 8);
      const u32x4 gq = {tlo | ((__float_as_uint(S[0]) + 0x80u) >> 8), thi, tlo | ((__float_as_uint(S[1]) + 0x80u) >> 8), thi};
      __builtin_amdgcn_raw_buffer_store_b128(gq, wsr, qout, 0, SC1);
    }
    SB();
    qsync(12);
    STAMP(4);
    if (wave == 0) {
      const unsigned qin = (unsigned)WS_Q + (unsigned)(kvh * 8 + b) * (4 * 512 * 8);
      const unsigned wlo = (unsigned)(epoch << 24), whi = (unsigned)(epoch >> 8);
      u32x4 g[16];
      unsigned spins = 0;
      __builtin_amdgcn_s_sleep(POLL_DELAY);
      for (;;) {
        asm volatile("" ::: "memory");
#pragma unroll
        for (int i = 0; i < 16; ++i) g[i] = __builtin_amdgcn_raw_buffer_load_b128(wsr, lane * 16 + i * 1024, qin, SC1);
        unsigned bad = 0;
#pragma unroll
        for (int i = 0; i < 16; ++i) bad |= ((g[i][0] ^ wlo) & 0xff000000u) | (g[i][1] ^ whi) | ((g[i][2] ^ wlo) & 0xff000000u) | (g[i][3] ^ whi);
        if (__all(bad == 0)) break;
        __builtin_amdgcn_s_sleep(1);
        if (++spins > SPIN_CAP) break;
      }
#pragma unroll
      for (int j = 0; j < 4; ++j) {
        float q0 = 0.f, q1 = 0.f;
#pragma unroll
        for (int k4 = 0; k4 < 4; ++k4) { q0 += __uint_as_float(g[k4 * 4 + j][0] << 8); q1 += __uint_as_float(g[k4 * 4 + j][2] << 8); }
        *reinterpret_cast<unsigned*>(smem + L_QB + (j * 128 + 2 * lane) * 2) = bfbits(q0) | (bfbits(q1) << 16);
      }
      __hip_atomic_store(qf, 1u, __ATOMIC_RELEASE, __HIP_MEMORY_SCOPE_WORKGROUP);
      STAMP(4);
    }
  }
  const size_t pair_off = (size_t)(b * HKV + kvh) * SK * DH * 2 + (size_t)s * 4 * (TK * DH * 2);
  const unsigned char* const kbase = reinterpret_cast<const unsigned char*>(prm.kc) + pair_off;
  const unsigned char* const vbase = reinterpret_cast<const unsigned char*>(prm.vc) + pair_off;
  auto tile_off = [](int T) { return (size_t)(unsigned)(((T >> 2) * 16 + (T & 3)) * (TK * DH * 2)); };
  const unsigned koff = r * 512 + c * 16, voff = lane * 16;
  u32x4 K[8], V[8], KB[8], VB[8];
  const bool streamer = wave >= NWV - NSW;
  constexpr int T0 = 2 * NWV - NSW;
  {
#define PACE_WAIT() do { SB(); asm volatile("s_waitcnt vmcnt(%0)" :: "i"(PACE - 4) : "memory"); SB(); } while (0)
    auto prefill = [&](u32x4 (&Kr)[8], u32x4 (&Vr)[8], auto SET) {
      constexpr int set = decltype(SET)::value;
      const unsigned char* kp = kbase + tile_off(set * NWV + wave);
      const unsigned char* vp = vbase + tile_off(set * NWV + wave);
      if constexpr (set * 16 >= PACE) PACE_WAIT();
#pragma unroll
      for (int i = 0; i < 4; ++i) Kr[i] = ldnt(kp + (i >> 1) * 2048 + (i & 1) * 256 + koff);
      if constexpr (set * 16 + 4 >= PACE) PACE_WAIT();
#pragma unroll
      for (int i = 4; i < 8; ++i) Kr[i] = ldnt(kp + (i >> 1) * 2048 + (i & 1) * 256 + koff);
      if constexpr (set * 16 + 8 >= PACE) PACE_WAIT();
#pragma unroll
      for (int i = 0; i < 4; ++i) Vr[i] = ldnt(vp + i * 1024 + voff);
      if constexpr (set * 16 + 12 >= PACE) PACE_WAIT();
#pragma unroll
      for (int i = 4; i < 8; ++i) Vr[i] = ldnt(vp + i * 1024 + voff);
      SB();
    };
    prefill(K, V, IC<0>{});
    if (!streamer) prefill(KB, VB, IC<1>{});
  }
  while (__hip_atomic_load(qf, __ATOMIC_ACQUIRE, __HIP_MEMORY_SCOPE_WORKGROUP) == 0u) __builtin_amdgcn_s_sleep(1);
  epoch = *reinterpret_cast<const u64*>(smem + L_EP);
  u32x4 q[2][2];
#pragma unroll
  for (int h = 0; h < 2; ++h)
#pragma unroll
    for (int j = 0; j < 2; ++j) q[h][j] = *reinterpret_cast<const u32x4*>(smem + L_QB + (h * 256 + (c + 16 * j) * 8) * 2);
  STAMP(5);

  float m = -INFINITY, l = 0.f;
  f32x2 acc[2][4];
#pragma unroll
  for (int h = 0; h < 2; ++h)
#pragma unroll
    for (int k = 0; k < 4; ++k) acc[h][k] = f32x2{0.f, 0.f};
  float* pl = reinterpret_cast<float*>(smem + L_PL) + wave * 32;
  const int pw = (r & 1) * 16 + ((c >> 2) * 2 + (r >> 1)) * 2 + ((c >> 1) & 1);
  constexpr float C2 = 0.0625f * 1.4426950408889634f;
  const int nrec = streamer ? (int)((size_t)SK * DH * 2 - (size_t)s * 4 * (TK * DH * 2)) : 0;
  const __amdgpu_buffer_rsrc_t krs = __builtin_amdgcn_make_buffer_rsrc(const_cast<unsigned char*>(kbase), 0, nrec, 0x00020000);
  const __amdgpu_buffer_rsrc_t vrs = __builtin_amdgcn_make_buffer_rsrc(const_cast<unsigned char*>(vbase), 0, nrec, 0x00020000);
  auto tile = [&](u32x4 (&Kr)[8], u32x4 (&Vr)[8], auto MODE, int T) {
    constexpr int mode = decltype(MODE)::value;
    const unsigned char* kp = kbase + tile_off(T);
    const unsigned char* vp = vbase + tile_off(T);
    float x[8];
#pragma unroll
    for (int g = 0; g < 4; ++g) {
#pragma unroll
      for (int h = 0; h < 2; ++h) {
        float d = 0.f;
#pragma unroll
        for (int j = 0; j < 2; ++j)
#pragma unroll
          for (int k = 0; k < 4; ++k) d = dot2(Kr[2 * g + j][k], q[h][j][k], d);
        x[2 * g + h] = d;
      }
      if constexpr (mode == 0) {
        Kr[2 * g] = ldnt(kp + g * 2048 + koff);
        Kr[2 * g + 1] = ldnt(kp + g * 2048 + 256 + koff);
      } else if constexpr (mode == 3) {
        Kr[2 * g] = __builtin_amdgcn_raw_buffer_load_b128(krs, koff + g * 2048, (unsigned)tile_off(T), 2);
        Kr[2 * g + 1] = __builtin_amdgcn_raw_buffer_load_b128(krs, koff + g * 2048 + 256, (unsigned)tile_off(T), 2);
      }
      SB();
    }
#define TR_STEP(HALF, CTRL, BIT) _Pragma("unroll") for (int i = 0; i < (HALF); ++i) { \
      const float lo_ = x[i], hi_ = x[i + (HALF)]; const float t1_ = lo_ + dpp<CTRL>(lo_), t2_ = hi_ + dpp<CTRL>(hi_); \
      x[i] = (c & (BIT)) ? t2_ : t1_; }
    TR_STEP(4, XOR8, 8) TR_STEP(2, XOR7, 4) TR_STEP(1, XOR2, 2)
#undef TR_STEP
    float sc = x[0] + dpp<XOR1>(x[0]);
    sc = rbf(sc) * C2;
    if (__any(sc > m)) {
      float pm = sc;
      pm = fmaxf(pm, dpp<ROR4>(pm)); pm = fmaxf(pm, dpp<XOR8>(pm));
      pm = x32_max(x16_max(pm));
      const float mn = fmaxf(m, pm);
      const float alpha = __builtin_amdgcn_exp2f(m - mn);
      m = mn; l *= alpha;
      const float a0 = dpp<0x00>(alpha), a1 = dpp<0xAA>(alpha);
#pragma unroll
      for (int k = 0; k < 4; ++k) { acc[0][k] *= a0; acc[1][k] *= a1; }
    }
    const float p = __builtin_amdgcn_exp2f(sc - m);
    l += p;
    pl[pw] = p;
    f32x4 ppv[4];
#pragma unroll
    for (int i2 = 0; i2 < 4; ++i2) ppv[i2] = *reinterpret_cast<const f32x4*>(pl + r2 * 16 + i2 * 4);
    SB();
#pragma unroll
    for (int i2 = 0; i2 < 4; ++i2) {
      const f32x4 pp = ppv[i2];
#pragma unroll
      for (int e = 0; e < 2; ++e) {
        const int i = 2 * i2 + e;
        const float p0 = pp[2 * e], p1 = pp[2 * e + 1];
#pragma unroll
        for (int k = 0; k < 4; ++k) {
          const f32x2 vv = {lo2f(Vr[i][k]), hi2f(Vr[i][k])};
          acc[0][k] = __builtin_elementwise_fma(f32x2{p0, p0}, vv, acc[0][k]);
          acc[1][k] = __builtin_elementwise_fma(f32x2{p1, p1}, vv, acc[1][k]);
        }
        asm volatile("" : "+v"(acc[0][0]), "+v"(acc[0][1]), "+v"(acc[0][2]), "+v"(acc[0][3]), "+v"(acc[1][0]), "+v"(acc[1][1]), "+v"(acc[1][2]), "+v"(acc[1][3]) :: "memory");
        if constexpr (mode == 0) Vr[i] = ldnt(vp + i * 1024 + voff);
        else if constexpr (mode == 3) Vr[i] = __builtin_amdgcn_raw_buffer_load_b128(vrs, voff + i * 1024, (unsigned)tile_off(T), 2);
        SB();
      }
    }
  };
  constexpr float KC = 0.7978845608028654f, L2E = 1.4426950408889634f;
  constexpr float K1 = -2.f * KC * L2E, K2 = -2.f * KC * 0.044715f * L2E;
  f32x2 c0[4], c1[4], c2c[4], c3[4], a0c[4], a1c[4];
  float wmax = 0.f, bmax = 0.f;
  auto mlp_constants = [&]() {
    const u16* mwp = reinterpret_cast<const u16*>(smem + L_MW);
#pragma unroll
    for (int i = 0; i < 7; ++i) {
      const int f = (tid & 255) + 256 * i;
      const float w = lo2f(mwp[f]), bb = lo2f(mwp[FF + f]), v = lo2f(mwp[2 * FF + f]);
      c3[i >> 1][i & 1] = K2 * w * w * w; c2c[i >> 1][i & 1] = 3.f * K2 * w * w * bb;
      c1[i >> 1][i & 1] = K1 * w + 3.f * K2 * w * bb * bb; c0[i >> 1][i & 1] = K1 * bb + K2 * bb * bb * bb;
      a1c[i >> 1][i & 1] = w * v; a0c[i >> 1][i & 1] = bb * v;
      wmax = fmaxf(wmax, fabsf(w)); bmax = fmaxf(bmax, fabsf(bb));
    }
    c0[3][1] = c1[3][1] = c2c[3][1] = c3[3][1] = a0c[3][1] = a1c[3][1] = 0.f;
#pragma unroll
    for (int i = 0; i < 4; ++i) asm volatile("" : "+v"(c0[i]), "+v"(c1[i]), "+v"(c2c[i]), "+v"(c3[i]), "+v"(a0c[i]), "+v"(a1c[i]));
    asm volatile("" : "+v"(wmax), "+v"(bmax));
  };
  tile(K, V, IC<3>{}, T0 + (wave & (NSW - 1)));
  if (!streamer) tile(KB, VB, IC<1>{}, 0);
  if (streamer) {
    for (int nxt = T0 + NSW + (wave & (NSW - 1)); nxt < NTW; nxt += NSW) {
      tile(K, V, IC<0>{}, nxt);
      asm volatile("s_waitcnt vmcnt(%0)" :: "i"(16 - DRAIN) : "memory");
    }
    tile(K, V, IC<1>{}, 0);
  }
  u32x4 BF[2][8];
  if (wave < 4) {
    if (wave == 3) {
      const u32x4 e4 = {EMPTY, EMPTY, EMPTY, EMPTY};
      __builtin_amdgcn_raw_buffer_store_b128(e4, wsr, prec + lane * 16, 0, SC1);
      __builtin_amdgcn_raw_buffer_store_b128(e4, wsr, prec + 1024 + lane * 16, 0, SC1);
      const unsigned yo = yout + (lane >= 32 ? 8 * DM * 4 : 0) + (lane & 15) * 16;
      __builtin_amdgcn_raw_buffer_store_b128(e4, wsr, lane < 16 ? prec + 2048 + lane * 16 : ((lane < 48 && jq < 28) ? yo : OOB), 0, SC1);
      asm volatile("s_waitcnt vmcnt(0)" ::: "memory");
      __builtin_amdgcn_raw_buffer_store_b64(__builtin_bit_cast(u32x2, epoch), wsr, lane == 0 ? (unsigned)WS_READY + bid * 8 : OOB, 0, SC1);
    }
    const u16* wp = prm.wo + (jq < 28 ? (size_t)(kvh * 512 + 128 * wave + 32 * ((lane >> 3) & 1) + 8 * r) * DM + cq * 64 : (size_t)0) + (lane & 7) * 8;
#pragma unroll
    for (int u = 0; u < 2; ++u) {
      u32x4 W[8];
#pragma unroll
      for (int i = 0; i < 8; ++i) W[i] = ldnt(wp + (64 * u + i) * DM);
#pragma unroll
      for (int p = 0; p < 4; ++p)
#pragma unroll
        for (int k = 0; k < 4; ++k) {
          BF[u][2 * k][p] = __builtin_amdgcn_perm(W[2 * p + 1][k], W[2 * p][k], 0x05040100u);
          BF[u][2 * k + 1][p] = __builtin_amdgcn_perm(W[2 * p + 1][k], W[2 * p][k], 0x07060302u);
        }
#pragma unroll
      for (int j = 0; j < 8; ++j) asm volatile("" : "+v"(BF[u][j]));
      SB();
    }
    {
      const int mi = tid < 224 ? tid : 0;
      const u32x4 m0 = ldnt(prm.w1 + mi * 8), m1 = ldnt(prm.b1 + mi * 8), m2 = ldnt(prm.w2 + mi * 8);
      const unsigned b2r = __builtin_nontemporal_load(prm.b2);
      *reinterpret_cast<unsigned*>(smem + L_EP + 8) = b2r;
      *reinterpret_cast<u32x4*>(smem + L_MW + mi * 16) = m0;
      *reinterpret_cast<u32x4*>(smem + L_MW + FF * 2 + mi * 16) = m1;
      *reinterpret_cast<u32x4*>(smem + L_MW + 2 * FF * 2 + mi * 16) = m2;
    }
    const unsigned char* rdyp = ws + WS_READY + tid * 8;
    unsigned spins = 0;
    while (!__all(gload(rdyp) == epoch) && ++spins < SPIN_CAP) __builtin_amdgcn_s_sleep(64);
  }
  STAMP(6);
  l += dpp<ROR4>(l); l += dpp<XOR8>(l); l = x32_sum(x16_sum(l));
  float* mrg = reinterpret_cast<float*>(smem + L_MRG);
  {
    float* rec = mrg + (wave * 2 + r2) * 260;
    float a8[8];
#pragma unroll
    for (int k = 0; k < 8; ++k) {
      const auto t = __builtin_amdgcn_permlane32_swap(__float_as_uint(acc[0][k >> 1][k & 1]), __float_as_uint(acc[1][k >> 1][k & 1]), false, false);
      a8[k] = __uint_as_float(t[0]) + __uint_as_float(t[1]);
    }
    *reinterpret_cast<f32x4*>(rec + 4 + c2 * 8) = f32x4{a8[0], a8[1], a8[2], a8[3]};
    *reinterpret_cast<f32x4*>(rec + 8 + c2 * 8) = f32x4{a8[4], a8[5], a8[6], a8[7]};
    *reinterpret_cast<f32x2*>(mrg + (wave * 2 + ((c >> 1) & 1)) * 260) = f32x2{m, l};
  }
  unsigned char* const tl = smem + L_T0;
  __syncthreads();
  STAMP(7);
  if (wave < 2) {
    f32x2 ml[NWV];
    f32x4 pa[NWV];
#pragma unroll
    for (int w = 0; w < NWV; ++w) { ml[w] = *reinterpret_cast<const f32x2*>(mrg + (w * 2 + wave) * 260); pa[w] = *reinterpret_cast<const f32x4*>(mrg + (w * 2 + wave) * 260 + 4 + lane * 4); }
    float M = ml[0][0];
#pragma unroll
    for (int w = 1; w < NWV; ++w) M = fmaxf(M, ml[w][0]);
    float L = 0.f;
    f32x4 on = {0.f, 0.f, 0.f, 0.f};
#pragma unroll
    for (int w = 0; w < NWV; ++w) {
      const float wt = __builtin_amdgcn_exp2f(ml[w][0] - M);
      L = fmaf(wt, ml[w][1], L);
      on += wt * pa[w];
    }
    on *= __builtin_amdgcn_rcpf(L);
    const float lse = M + __builtin_amdgcn_logf(L);
    const u32x4 ob = {pubbits(on[0]), pubbits(on[1]), pubbits(on[2]), pubbits(on[3])};
    __builtin_amdgcn_raw_buffer_store_b128(ob, wsr, prec + tid * 16, 0, SC1);
    __builtin_amdgcn_raw_buffer_store_b32(pubbits(lse), wsr, lane == 0 ? prec + 2048 + wave * 128 : OOB, 0, SC1);
  }
  if (bid == 0 && tid == 448) gstore(ws + WS_EPOCH, epoch);
  STAMP(8);
  mlp_constants();
  if (jq < 28) {
    if (wave >= 4) {
      const int beta = (wave >> 1) & 1, h = wave & 1;
      u32x4 sa[4]; unsigned sl[4];
      unsigned spins = 0;
      for (;;) {
        asm volatile("" ::: "memory");
#pragma unroll
        for (int t = 0; t < 4; ++t) {
          const unsigned rec = (unsigned)WS_P + (unsigned)(t * 64 + (2 * bg + beta) * 8 + kvh) * PREC * 4;
          sa[t] = __builtin_amdgcn_raw_buffer_load_b128(wsr, h * 1024 + lane * 16, rec, SC1);
          sl[t] = __builtin_amdgcn_raw_buffer_load_b32(wsr, 2048 + h * 128, rec, SC1);
        }
        bool ok = true;
#pragma unroll
        for (int t = 0; t < 4; ++t) ok &= full4(sa[t]) && sl[t] != EMPTY;
        if (__all(ok)) break;
        __builtin_amdgcn_s_sleep(SPIN_SLEEP);
        if (++spins > SPIN_CAP) break;
      }
      const float l0 = __uint_as_float(sl[0]), l1 = __uint_as_float(sl[1]), l2 = __uint_as_float(sl[2]), l3 = __uint_as_float(sl[3]);
      const float MM = fmaxf(fmaxf(l0, l1), fmaxf(l2, l3));
      const float e0 = __builtin_amdgcn_exp2f(l0 - MM), e1 = __builtin_amdgcn_exp2f(l1 - MM), e2 = __builtin_amdgcn_exp2f(l2 - MM), e3 = __builtin_amdgcn_exp2f(l3 - MM);
      const float inv = __builtin_amdgcn_rcpf((e0 + e1) + (e2 + e3));
      const f32x4 oo = (e0 * __builtin_bit_cast(f32x4, sa[0]) + e1 * __builtin_bit_cast(f32x4, sa[1]) + e2 * __builtin_bit_cast(f32x4, sa[2]) + e3 * __builtin_bit_cast(f32x4, sa[3])) * inv;
      *reinterpret_cast<u32x2*>(tl + T_OB + (tid & 255) * 8) = u32x2{bfbits(oo[0]) | (bfbits(oo[1]) << 16), bfbits(oo[2]) | (bfbits(oo[3]) << 16)};
    }
    STAMP(9);
    __syncthreads();
    float* red = reinterpret_cast<float*>(tl + T_RED2);
    if (wave < 4) {
      const bf16x8 a0 = *reinterpret_cast<const bf16x8*>(tl + T_OB + (((c >> 1) & 1) * 512 + 128 * wave + 32 * (c & 1) + 8 * r) * 2);
      const bf16x8 a1 = *reinterpret_cast<const bf16x8*>(tl + T_OB + (((c >> 1) & 1) * 512 + 128 * wave + 64 + 32 * (c & 1) + 8 * r) * 2);
      f32x4 y0[2], y1[2];
#pragma unroll
      for (int j = 0; j < 8; ++j) {
        f32x4 d = __builtin_amdgcn_mfma_f32_16x16x32_bf16(a0, __builtin_bit_cast(bf16x8, BF[0][j]), f32x4{0.f, 0.f, 0.f, 0.f}, 0, 0, 0);
        d = __builtin_amdgcn_mfma_f32_16x16x32_bf16(a1, __builtin_bit_cast(bf16x8, BF[1][j]), d, 0, 0, 0);
        y0[j >> 2][j & 3] = (c & 8) ? d[1] : d[0];
        y1[j >> 2][j & 3] = (c & 8) ? d[3] : d[2];
      }
      float* o = red + (wave * 2 + (c >> 3)) * 128 + (c & 7) * 8;
      *reinterpret_cast<f32x4*>(o) = y0[0]; *reinterpret_cast<f32x4*>(o + 4) = y0[1];
      *reinterpret_cast<f32x4*>(o + 64) = y1[0]; *reinterpret_cast<f32x4*>(o + 68) = y1[1];
    }
    STAMP(10);
    __syncthreads();
    if (wave == 7) {
      f32x4 t = *reinterpret_cast<const f32x4*>(red + (r2 * 4) * 128 + c2 * 4);
#pragma unroll
      for (int w = 1; w < 4; ++w) t += *reinterpret_cast<const f32x4*>(red + (r2 * 4 + w) * 128 + c2 * 4);
      const u32x4 yb = {pubbits(x32_sum(t[0])), pubbits(x32_sum(t[1])), pubbits(x32_sum(t[2])), pubbits(x32_sum(t[3]))};
      __builtin_amdgcn_raw_buffer_store_b128(yb, wsr, lane < 32 ? yout + (lane >> 4) * (8 * DM * 4) + c * 16 : OOB, 0, SC1);
    }
  }
  STAMP(11);
  float* hs = reinterpret_cast<float*>(tl + T_HS);
  float hv = 0.f;
  if (wave < 7) {
    const float xres = lo2f(reinterpret_cast<const u16*>(smem + L_XR)[tid]);
    const unsigned yin = (unsigned)WS_Y + (unsigned)b * (8 * DM * 4);
    unsigned yv[8];
    unsigned spins = 0;
    for (;;) {
      asm volatile("" ::: "memory");
#pragma unroll
      for (int k = 0; k < 8; ++k) yv[k] = __builtin_amdgcn_raw_buffer_load_b32(wsr, tid * 4 + k * (DM * 4), yin, SC1);
      unsigned mx = yv[0];
#pragma unroll
      for (int k = 1; k < 8; ++k) mx = max(mx, yv[k]);
      if (__all(mx != EMPTY)) break;
      __builtin_amdgcn_s_sleep(SPIN_SLEEP);
      if (++spins > SPIN_CAP) break;
    }
    const float t = ((__uint_as_float(yv[0]) + __uint_as_float(yv[1])) + (__uint_as_float(yv[2]) + __uint_as_float(yv[3]))) +
                    ((__uint_as_float(yv[4]) + __uint_as_float(yv[5])) + (__uint_as_float(yv[6]) + __uint_as_float(yv[7])));
    hv = rbf(xres + rbf(t));
    hs[tid] = hv;
  }
  STAMP(12);
  const int jo = kvh * 4 + s, par = wave >> 2;
  float mbs[2], rds[2];
  {
    const float cw = __builtin_bit_cast(float, __builtin_amdgcn_readfirstlane(__builtin_bit_cast(int, hv)));
    const float dv = hv - cw;
    float s1 = dv, s2 = dv * dv;
    wave_sum2(s1, s2);
    f32x2* stp = reinterpret_cast<f32x2*>(tl + T_ST);
    stp[wave] = f32x2{fmaf(s1, 1.f / 64.f, cw), fmaf(-s1 * (1.f / 64.f), s1, s2)};
    __syncthreads();
    f32x2 sv[7];
#pragma unroll
    for (int w = 0; w < 7; ++w) sv[w] = stp[w];
    const f32x2 tot = ((sv[0] + sv[1]) + (sv[2] + sv[3])) + ((sv[4] + sv[5]) + sv[6]);
    const float mean = tot[0] * (1.f / 7.f);
    float dm0 = 0.f, dm1 = 0.f;
#pragma unroll
    for (int w = 0; w < 7; ++w) { const float d = sv[w][0] - mean; if (w & 1) dm1 = fmaf(d, d, dm1); else dm0 = fmaf(d, d, dm0); }
    mbs[0] = rbf(mean); rds[0] = ln_rden(fmaf(64.f, dm0 + dm1, tot[1]) * (1.f / DM));
    hv = wave < 7 ? rbf(rbf(hv - mbs[0]) * rds[0]) : 0.f;
  }
  {
    float s1 = hv, s2 = hv * hv;
    wave_sum2(s1, s2);
    f32x2* stp = reinterpret_cast<f32x2*>(tl + T_ST + 64);
    stp[wave] = f32x2{s1, s2};
    __syncthreads();
    f32x2 sv[7];
#pragma unroll
    for (int w = 0; w < 7; ++w) sv[w] = stp[w];
    const f32x2 tot = (((sv[0] + sv[1]) + (sv[2] + sv[3])) + ((sv[4] + sv[5]) + sv[6])) * (1.f / DM);
    mbs[1] = rbf(tot[0]); rds[1] = ln_rden(fmaxf(fmaf(-tot[0], tot[0], tot[1]), 0.f));
  }
  float g1 = hs[14 * jo + 2 * (lane < 7 ? lane : 6) + par];
  g1 = rbf(rbf(g1 - mbs[0]) * rds[0]);
  g1 = rbf(rbf(g1 - mbs[1]) * rds[1]);
  const float g2 = g1 * g1, g3 = g2 * g1;
  STAMP(13);
  float ma[7];
  auto mlp = [&](auto SAFE) {
    constexpr bool safe = decltype(SAFE)::value != 0;
#pragma unroll
    for (int mo = 0; mo < 7; ++mo) {
      const float h1 = __builtin_bit_cast(float, __builtin_amdgcn_readlane(__builtin_bit_cast(int, g1), mo));
      const float h2 = __builtin_bit_cast(float, __builtin_amdgcn_readlane(__builtin_bit_cast(int, g2), mo));
      const float h3 = __builtin_bit_cast(float, __builtin_amdgcn_readlane(__builtin_bit_cast(int, g3), mo));
      const f32x2 H1 = {h1, h1}, H2 = {h2, h2}, H3 = {h3, h3};
      f32x2 d[4], a[4];
#pragma unroll
      for (int i = 0; i < 4; ++i) {
        const f32x2 z = __builtin_elementwise_fma(c3[i], H3, __builtin_elementwise_fma(c2c[i], H2, __builtin_elementwise_fma(c1[i], H1, c0[i])));
        a[i] = __builtin_elementwise_fma(a1c[i], H1, a0c[i]);
        d[i][0] = __builtin_amdgcn_exp2f(z[0]);
        d[i][1] = i < 3 ? __builtin_amdgcn_exp2f(z[1]) : 0.f;
        d[i] += 1.f;
      }
      float acc1 = a[3][0] * __builtin_amdgcn_rcpf(d[3][0]);
#pragma unroll
      for (int i = 0; i < 3; ++i) {
        if constexpr (safe) { acc1 = fmaf(a[i][0], __builtin_amdgcn_rcpf(d[i][0]), acc1); acc1 = fmaf(a[i][1], __builtin_amdgcn_rcpf(d[i][1]), acc1); }
        else acc1 = fmaf(fmaf(a[i][0], d[i][1], a[i][1] * d[i][0]), __builtin_amdgcn_rcpf(d[i][0] * d[i][1]), acc1);
      }
      ma[mo] = acc1;
    }
  };
  {
    float hm = fabsf(g1);
    hm = fmaxf(hm, dpp<XOR1>(hm)); hm = fmaxf(hm, dpp<XOR2>(hm)); hm = fmaxf(hm, dpp<XOR7>(hm));
    hm = __builtin_bit_cast(float, __builtin_amdgcn_readfirstlane(__builtin_bit_cast(int, hm)));
    if (__all(fmaf(hm, wmax, bmax) <= 7.5f)) mlp(IC<0>{}); else mlp(IC<1>{});
  }
  STAMP(14);
  {
    float x[8] = {ma[0], ma[1], ma[2], ma[3], ma[4], ma[5], ma[6], 0.f};
#define TR_STEP(HALF, CTRL, BIT) _Pragma("unroll") for (int i = 0; i < (HALF); ++i) { \
      const float lo_ = x[i], hi_ = x[i + (HALF)]; const float t1_ = lo_ + dpp<CTRL>(lo_), t2_ = hi_ + dpp<CTRL>(hi_); \
      x[i] = (c & (BIT)) ? t2_ : t1_; }
    TR_STEP(4, XOR8, 8) TR_STEP(2, XOR7, 4) TR_STEP(1, XOR2, 2)
#undef TR_STEP
    float t = x[0] + dpp<XOR1>(x[0]);
    t = x32_sum(x16_sum(t));
    float* red3 = reinterpret_cast<float*>(tl + T_RED3);
    red3[wave * 8 + (c >> 1)] = t;
    __syncthreads();
    if (wave == 0) {
      const int to = lane < 14 ? lane : 13;
      const int mo = to >> 1, pr = to & 1;
      const float y = (red3[(pr * 4 + 0) * 8 + mo] + red3[(pr * 4 + 1) * 8 + mo]) + (red3[(pr * 4 + 2) * 8 + mo] + red3[(pr * 4 + 3) * 8 + mo]);
      const int d = 14 * jo + to;
      const float y2 = rbf(rbf(y) + lo2f(*reinterpret_cast<const unsigned*>(smem + L_EP + 8)));
      const u16 ov = (u16)bfbits(hs[d] + y2);
      if (lane < 14) prm.out[b * DM + d] = ov;
    }
  }
  STAMP(15);
}
}

extern "C" void kernel_launch(void* const* d_in, const int* in_sizes, int n_in, void* d_out, int out_size, void* d_ws, size_t ws_size,
                              hipStream_t stream) {
  Params p;
  p.x = (const u16*)d_in[0]; p.kc = (const u16*)d_in[1]; p.vc = (const u16*)d_in[2]; p.wq = (const u16*)d_in[3]; p.wo = (const u16*)d_in[4];
  p.w1 = (const u16*)d_in[6]; p.b1 = (const u16*)d_in[7]; p.w2 = (const u16*)d_in[8]; p.b2 = (const u16*)d_in[9];
  p.out = (u16*)d_out; p.ws = (unsigned char*)d_ws;
  k_fused<<<NWG, NT, 0, stream>>>(p);
}
```

```cpp
#include <hip/hip_runtime.h>
#include <hip/hip_bf16.h>
#include <stdint.h>
#include <type_traits>

namespace {
constexpr int NB = 8, DM = 448, HQ = 16, HKV = 8, SK = 8192, DH = 256, FF = 1792;
constexpr int NWG = 256, NT = 512, NWV = 8;
constexpr int NSPLIT = 4;
constexpr int TK = 16;

typedef unsigned u32x4 __attribute__((ext_vector_type(4)));
typedef float f32x4 __attribute__((ext_vector_type(4)));
typedef float f32x2 __attribute__((ext_vector_type(2)));
typedef __bf16 bf16x2 __attribute__((ext_vector_type(2)));
typedef __bf16 bf16x8 __attribute__((ext_vector_type(8)));
typedef short s16x4 __attribute__((ext_vector_type(4)));
typedef short s16x8 __attribute__((ext_vector_type(8)));
typedef unsigned short u16;
typedef unsigned long long u64;
#define LAS __attribute__((address_space(3)))
#define GAS __attribute__((address_space(1)))
typedef GAS u64 gu64;
#define RLX_AGENT __ATOMIC_RELAXED, __HIP_MEMORY_SCOPE_AGENT

__device__ __forceinline__ float rbf(float x) { return (float)(__bf16)x; }
__device__ __forceinline__ unsigned bfbits(float x) { return (unsigned)__builtin_bit_cast(u16, (__bf16)x); }
__device__ __forceinline__ float lo2f(unsigned u) { return __builtin_bit_cast(float, u << 16); }
__device__ __forceinline__ float hi2f(unsigned u) { return __builtin_bit_cast(float, u & 0xffff0000u); }
__device__ __forceinline__ float dot2(unsigned k, unsigned q, float acc) {
  return __builtin_amdgcn_fdot2_f32_bf16(__builtin_bit_cast(bf16x2, k), __builtin_bit_cast(bf16x2, q), acc, false);
}
template <int CTRL> __device__ __forceinline__ float dpp(float x) {
  return __builtin_bit_cast(float, __builtin_amdgcn_mov_dpp(__builtin_bit_cast(int, x), CTRL, 0xf, 0xf, true));
}
constexpr int XOR1 = 0xB1, XOR2 = 0x4E, XOR7 = 0x141, XOR8 = 0x128, ROR4 = 0x124;
__device__ __forceinline__ float x16_sum(float x) {
  auto s = __builtin_amdgcn_permlane16_swap(__float_as_uint(x), __float_as_uint(x), false, false);
  return __uint_as_float(s[0]) + __uint_as_float(s[1]);
}
__device__ __forceinline__ float x32_sum(float x) {
  auto t = __builtin_amdgcn_permlane32_swap(__float_as_uint(x), __float_as_uint(x), false, false);
  return __uint_as_float(t[0]) + __uint_as_float(t[1]);
}
__device__ __forceinline__ float x16_max(float x) {
  auto s = __builtin_amdgcn_permlane16_swap(__float_as_uint(x), __float_as_uint(x), false, false);
  return fmaxf(__uint_as_float(s[0]), __uint_as_float(s[1]));
}
__device__ __forceinline__ float x32_max(float x) {
  auto t = __builtin_amdgcn_permlane32_swap(__float_as_uint(x), __float_as_uint(x), false, false);
  return fmaxf(__uint_as_float(t[0]), __uint_as_float(t[1]));
}
__device__ __forceinline__ float wave_sum(float x) {
  x += dpp<XOR1>(x); x += dpp<XOR2>(x); x += dpp<XOR7>(x); x += dpp<XOR8>(x);
  return x32_sum(x16_sum(x));
}
__device__ __forceinline__ void wave_sum2(float& a, float& b) {
#define WS2(E) { const float ta = E(a), tb = E(b); a = ta; b = tb; asm volatile("" : "+v"(a), "+v"(b)); }
  auto s1 = [](float x) { return x + dpp<XOR1>(x); }; auto s2 = [](float x) { return x + dpp<XOR2>(x); };
  auto s3 = [](float x) { return x + dpp<XOR7>(x); }; auto s4 = [](float x) { return x + dpp<XOR8>(x); };
  WS2(s1) WS2(s2) WS2(s3) WS2(s4)
#undef WS2
  { const auto t = __builtin_amdgcn_permlane16_swap(__float_as_uint(a), __float_as_uint(b), false, false);
    float u = __uint_as_float(t[0]) + __uint_as_float(t[1]);
    u = x32_sum(u);
    a = __builtin_bit_cast(float, __builtin_amdgcn_readlane(__builtin_bit_cast(int, u), 0));
    b = __builtin_bit_cast(float, __builtin_amdgcn_readlane(__builtin_bit_cast(int, u), 16)); }
}
__device__ __forceinline__ u32x4 ldnt(const void* p) { return __builtin_nontemporal_load(reinterpret_cast<const u32x4*>(p)); }
__device__ __forceinline__ u64 gload(const unsigned char* p) { return __hip_atomic_load((gu64*)p, RLX_AGENT); }
__device__ __forceinline__ void gstore(unsigned char* p, u64 v) { __hip_atomic_store((gu64*)p, v, RLX_AGENT); }
#define SB() __builtin_amdgcn_sched_barrier(0)

constexpr size_t WS_EPOCH = 0;
constexpr size_t WS_READY = 4096;
constexpr size_t WS_Q = 8192;
constexpr int PREC = 576;
constexpr size_t WS_P = WS_Q + (size_t)64 * 4 * 512 * 8;
constexpr size_t WS_Y = WS_P + (size_t)NWG * PREC * 4;
constexpr size_t WS_END = WS_Y + (size_t)NB * 8 * DM * 4;
constexpr unsigned SPIN_CAP = 1u << 20;
constexpr unsigned EMPTY = 0xffffffffu;
constexpr int NTW = SK / TK / NSPLIT;

constexpr int L_WSL = 0;
constexpr int L_RED = L_WSL + 8 * 2048;
constexpr int L_QB = L_RED + 4 * 4096;
constexpr int L_SP = 0;
constexpr int L_PL = L_QB + 1024;
constexpr int L_MRG = L_PL + 1024;
constexpr int L_MW = L_MRG + NWV * 2 * 260 * 4;
constexpr int L_XR = L_MW + 3 * FF * 2;
constexpr int L_EP = L_XR + 896;
constexpr int L_ST = L_EP + 16;
constexpr int L_QF = L_ST + 64;
constexpr int L_DUMMY = L_QF + 32;
constexpr int L_T0 = L_DUMMY + 16;
constexpr int T_OB = 0;
constexpr int T_RED2 = T_OB + 2048;
constexpr int T_HS = T_RED2 + 8192;
constexpr int T_RED3 = T_HS + 2048;
constexpr int T_ST = T_RED3 + 256;
constexpr int T_END = T_ST + 128;
constexpr int L_END = L_T0 + T_END;
constexpr unsigned OOB = 0x40000000u;
constexpr int NSW = 2;
constexpr int CAP = 12;
constexpr int GL = 2;
constexpr int XR = 2;
constexpr int PACE = 12;
constexpr int POLL_DELAY = 8, SPIN_SLEEP = 2;

struct Params {
  const u16* x; const u16* kc; const u16* vc; const u16* wq; const u16* wo;
  const u16* w1; const u16* b1; const u16* w2; const u16* b2;
  u16* out; unsigned char* ws;
};

typedef unsigned u32x2 __attribute__((ext_vector_type(2)));
__device__ __forceinline__ unsigned pubbits(float v) { return min(__float_as_uint(v), 0xfffffffeu); }
__device__ __forceinline__ bool full4(u32x4 v) { return max(max(v[0], v[1]), max(v[2], v[3])) != EMPTY; }
__device__ __forceinline__ float ln_rden(float var) { return __builtin_amdgcn_rcpf(rbf(__builtin_amdgcn_sqrtf(rbf(rbf(var) + 1e-5f)))); }
template <int N> using IC = std::integral_constant<int, N>;

__global__ void __launch_bounds__(NT, 2) k_fused(Params prm) {
  __shared__ __attribute__((aligned(16))) unsigned char smem[L_END];
  unsigned char* const ws = prm.ws;
  const int tid = threadIdx.x, lane = tid & 63, wave = __builtin_amdgcn_readfirstlane(tid >> 6);
  const int bid = blockIdx.x, kvh = bid & 7, b = (bid >> 3) & 7, s = bid >> 6, jq = bid >> 3;
  const int r = lane >> 4, c = lane & 15, r2 = lane >> 5, c2 = lane & 31;
  const __amdgpu_buffer_rsrc_t wsr = __builtin_amdgcn_make_buffer_rsrc(ws, 0, (int)WS_END, 0x00020000);
  constexpr int SC1 = 16;
  const unsigned prec = (unsigned)WS_P + (unsigned)bid * PREC * 4;
  const int bg = jq / 7, cq = jq - bg * 7;
  const unsigned yout = (unsigned)WS_Y + (unsigned)(((2 * bg) * 8 + kvh) * DM + cq * 64) * 4;
  const int rg = jq >> 3, cg = jq & 7;
  u32x4 xv[2], wv[2][2]; u32x2 xa[2]; u64 ep0;
  const unsigned qout = (unsigned)WS_Q + (unsigned)(((kvh * 8 + 2 * (wave & 3) + r2) * 4 + rg) * 512 + cg * 64 + 2 * c2) * 8;
  if (wave < 4) {
#pragma unroll
    for (int e = 0; e < 2; ++e) {
      const int ks = (wave + 4 * e) < 7 ? wave + 4 * e : 6;
      xv[e] = ldnt(prm.x + (wave + 4 * e) * DM + (lane < 56 ? lane : 55) * 8);
      xa[e] = __builtin_nontemporal_load(reinterpret_cast<const u32x2*>(prm.x + (c & 7) * DM + rg * 112 + 16 * ks + 4 * r));
      const u16* wqp = prm.wq + (size_t)(rg * 112 + 16 * ks + (lane >> 3)) * 4096 + kvh * 512 + cg * 64 + (lane & 7) * 8;
      wv[e][0] = ldnt(wqp);
      wv[e][1] = ldnt(wqp + 8 * 4096);
    }
    ep0 = gload(ws + WS_EPOCH);
  }
  *reinterpret_cast<u32x4*>(smem + L_QF + (lane & 1) * 16) = u32x4{0u, 0u, 0u, 0u};
  SB();
  __syncthreads();
  SB();
  u64 epoch = 0;
  LAS unsigned* const qf = (LAS unsigned*)(smem + L_QF);
  if (wave < 4) {
    auto qsync = [&](unsigned target) {
      asm volatile("s_waitcnt lgkmcnt(0)" ::: "memory");
      if (lane == 0) __hip_atomic_fetch_add(qf + 1, 1u, __ATOMIC_RELAXED, __HIP_MEMORY_SCOPE_WORKGROUP);
      while (__hip_atomic_load(qf + 1, __ATOMIC_RELAXED, __HIP_MEMORY_SCOPE_WORKGROUP) < target) {}
      asm volatile("" ::: "memory");
    };
    u16* wsl = reinterpret_cast<u16*>(smem + L_WSL) + wave * 2048;
    float* red = reinterpret_cast<float*>(smem + L_RED);
#pragma unroll
    for (int e = 0; e < 2; ++e) {
      *reinterpret_cast<u32x4*>(wsl + e * 1024 + (lane >> 3) * 64 + (lane & 7) * 8) = wv[e][0];
      *reinterpret_cast<u32x4*>(wsl + e * 1024 + (8 + (lane >> 3)) * 64 + (lane & 7) * 8) = wv[e][1];
    }
#pragma unroll
    for (int e = 0; e < 2; ++e) {
      float xf[8];
#pragma unroll
      for (int k = 0; k < 4; ++k) { xf[2 * k] = lo2f(xv[e][k]); xf[2 * k + 1] = hi2f(xv[e][k]); }
      float sm = 0.f;
#pragma unroll
      for (int k = 0; k < 8; ++k) sm += xf[k];
      if (lane >= 56) sm = 0.f;
      const float mean = wave_sum(sm) * (1.f / DM);
      float vs = 0.f;
#pragma unroll
      for (int k = 0; k < 8; ++k) { const float d = xf[k] - mean; vs += d * d; }
      if (lane >= 56) vs = 0.f;
      const float var = wave_sum(vs) * (1.f / DM);
      *reinterpret_cast<f32x2*>(smem + L_ST + (wave + 4 * e) * 8) = f32x2{rbf(mean), ln_rden(var)};
      *reinterpret_cast<u32x4*>(smem + ((wave + 4 * e == b && lane < 56) ? L_XR + lane * 16 : L_DUMMY)) = xv[e];
    }
    s16x4 bfr[2][4];
#pragma unroll
    for (int e = 0; e < 2; ++e)
#pragma unroll
      for (int ct = 0; ct < 4; ++ct) bfr[e][ct] = __builtin_amdgcn_ds_read_tr16_b64_v4i16((LAS s16x4*)(wsl + e * 1024 + (4 * r + (c >> 2)) * 64 + ct * 16 + (c & 3) * 4));
    asm volatile("" : "+v"(ep0));
    epoch = ep0 + 1ull;
    *reinterpret_cast<u64*>(smem + L_EP) = epoch;
    qsync(4);
    {
      const f32x2 st = *reinterpret_cast<const f32x2*>(smem + L_ST + (c & 7) * 8);
      u32x2 av[2];
#pragma unroll
      for (int e = 0; e < 2; ++e)
#pragma unroll
        for (int k = 0; k < 2; ++k) av[e][k] = bfbits(rbf(lo2f(xa[e][k]) - st[0]) * st[1]) | (bfbits(rbf(hi2f(xa[e][k]) - st[0]) * st[1]) << 16);
      if (wave == 3) av[1] = u32x2{0u, 0u};
#pragma unroll
      for (int ct = 0; ct < 4; ++ct) {
        f32x4 d = __builtin_amdgcn_mfma_f32_16x16x16bf16_1k(__builtin_bit_cast(s16x4, av[0]), bfr[0][ct], f32x4{0.f, 0.f, 0.f, 0.f}, 0, 0, 0);
        d = __builtin_amdgcn_mfma_f32_16x16x16bf16_1k(__builtin_bit_cast(s16x4, av[1]), bfr[1][ct], d, 0, 0, 0);
        float* o = red + wave * 1024 + 4 * r * 64 + ct * 16 + c;
        o[0] = d[0]; o[64] = d[1]; o[128] = d[2]; o[192] = d[3];
      }
    }
    qsync(8);
    {
      const int bq = 2 * wave + r2;
      const f32x2* rp = reinterpret_cast<const f32x2*>(red + bq * 64 + 2 * c2);
      const f32x2 S = (rp[0] + rp[512]) + (rp[1024] + rp[1536]);
      const unsigned tlo = (unsigned)(epoch << 24), thi = (unsigned)(epoch >> 8);
      const u32x4 gq = {tlo | ((__float_as_uint(S[0]) + 0x80u) >> 8), thi, tlo | ((__float_as_uint(S[1]) + 0x80u) >> 8), thi};
      __builtin_amdgcn_raw_buffer_store_b128(gq, wsr, qout, 0, SC1);
    }
    SB();
    qsync(12);
    if (wave == 0) {
      const unsigned qin = (unsigned)WS_Q + (unsigned)(kvh * 8 + b) * (4 * 512 * 8);
      const unsigned wlo = (unsigned)(epoch << 24), whi = (unsigned)(epoch >> 8);
      u32x4 g[16];
      unsigned spins = 0;
      __builtin_amdgcn_s_sleep(POLL_DELAY);
      for (;;) {
        asm volatile("" ::: "memory");
#pragma unroll
        for (int i = 0; i < 16; ++i) g[i] = __builtin_amdgcn_raw_buffer_load_b128(wsr, lane * 16 + i * 1024, qin, SC1);
        unsigned bad = 0;
#pragma unroll
        for (int i = 0; i < 16; ++i) bad |= ((g[i][0] ^ wlo) & 0xff000000u) | (g[i][1] ^ whi) | ((g[i][2] ^ wlo) & 0xff000000u) | (g[i][3] ^ whi);
        if (__all(bad == 0)) break;
        __builtin_amdgcn_s_sleep(1);
        if (++spins > SPIN_CAP) break;
      }
#pragma unroll
      for (int j = 0; j < 4; ++j) {
        float q0 = 0.f, q1 = 0.f;
#pragma unroll
        for (int k4 = 0; k4 < 4; ++k4) { q0 += __uint_as_float(g[k4 * 4 + j][0] << 8); q1 += __uint_as_float(g[k4 * 4 + j][2] << 8); }
        *reinterpret_cast<unsigned*>(smem + L_QB + (j * 128 + 2 * lane) * 2) = bfbits(q0) | (bfbits(q1) << 16);
      }
      __hip_atomic_store(qf, 1u, __ATOMIC_RELEASE, __HIP_MEMORY_SCOPE_WORKGROUP);
    }
  }
  const size_t pair_off = (size_t)(b * HKV + kvh) * SK * DH * 2 + ((size_t)s << GL) * (TK * DH * 2);
  const unsigned char* const kbase = reinterpret_cast<const unsigned char*>(prm.kc) + pair_off;
  const unsigned char* const vbase = reinterpret_cast<const unsigned char*>(prm.vc) + pair_off;
  const int rot = (b * HKV + kvh) * 3;
  auto tile_off = [&](int T) { return (size_t)(unsigned)((((((T >> GL) + rot) & ((NTW >> GL) - 1)) << (GL + 2)) + (T & ((1 << GL) - 1))) * (TK * DH * 2)); };
  const unsigned koff = r * 512 + c * 16, voff = lane * 16;
  u32x4 R0[8], R1[8], R2[8], R3[8];
  const bool streamer = wave >= NWV - NSW;
  const int sw = wave & (NSW - 1);
  constexpr int KH = 4 * (NWV - NSW);
  constexpr int VEND = NTW - 2 * (NWV - NSW);
#define PACE_WAIT() do { SB(); asm volatile("s_waitcnt vmcnt(%0)" :: "i"(PACE - 4) : "memory"); SB(); } while (0)
  auto kload = [&](u32x4 (&R)[8], auto POS, int T) {
    constexpr int pos = decltype(POS)::value;
    const unsigned char* kp = kbase + tile_off(T);
    if constexpr (pos >= PACE) PACE_WAIT();
#pragma unroll
    for (int i = 0; i < 4; ++i) R[i] = ldnt(kp + (i >> 1) * 2048 + (i & 1) * 256 + koff);
    if constexpr (pos + 4 >= PACE) PACE_WAIT();
#pragma unroll
    for (int i = 4; i < 8; ++i) R[i] = ldnt(kp + (i >> 1) * 2048 + (i & 1) * 256 + koff);
    SB();
  };
  auto vload = [&](u32x4 (&R)[8], int T) {
    const unsigned char* vp = vbase + tile_off(T);
#pragma unroll
    for (int i = 0; i < 8; ++i) R[i] = ldnt(vp + i * 1024 + voff);
    SB();
  };
  const int t0 = streamer ? KH + sw : 4 * wave, td = streamer ? NSW : 1;
  kload(R0, IC<0>{}, t0);
  kload(R1, IC<8>{}, t0 + td);
  if (!streamer) { kload(R2, IC<16>{}, t0 + 2); kload(R3, IC<24>{}, t0 + 3); }
  while (__hip_atomic_load(qf, __ATOMIC_ACQUIRE, __HIP_MEMORY_SCOPE_WORKGROUP) == 0u) __builtin_amdgcn_s_sleep(1);
  epoch = *reinterpret_cast<const u64*>(smem + L_EP);
  u32x4 q[2][2];
#pragma unroll
  for (int h = 0; h < 2; ++h)
#pragma unroll
    for (int j = 0; j < 2; ++j) q[h][j] = *reinterpret_cast<const u32x4*>(smem + L_QB + (h * 256 + (c + 16 * j) * 8) * 2);

  float mx = -INFINITY;
  f32x2 acc[2][4];
#pragma unroll
  for (int h = 0; h < 2; ++h)
#pragma unroll
    for (int k = 0; k < 4; ++k) acc[h][k] = f32x2{0.f, 0.f};
  float* const sp = reinterpret_cast<float*>(smem + L_SP);
  const int pw = (r & 1) * 16 + ((c >> 2) * 2 + (r >> 1)) * 2 + ((c >> 1) & 1);
  constexpr float C2 = 0.0625f * 1.4426950408889634f;
  const int nrec = streamer ? (int)((size_t)SK * DH * 2 - ((size_t)s << GL) * (TK * DH * 2)) : 0;
  const __amdgpu_buffer_rsrc_t krs = __builtin_amdgcn_make_buffer_rsrc(const_cast<unsigned char*>(kbase), 0, nrec, 0x00020000);
#define CAP_WAIT(n) asm volatile("s_waitcnt vmcnt(%0)" :: "i"(CAP - (n)) : "memory")
  auto ktile = [&](u32x4 (&R)[8], auto MODE, int T, int TN) {
    constexpr int mode = decltype(MODE)::value;
    const unsigned char* kp = kbase + tile_off(TN);
    const unsigned char* vp = vbase + tile_off(TN);
    float x[8];
#pragma unroll
    for (int g = 0; g < 4; ++g) {
#pragma unroll
      for (int h = 0; h < 2; ++h) {
        float d = 0.f;
#pragma unroll
        for (int j = 0; j < 2; ++j)
#pragma unroll
          for (int k = 0; k < 4; ++k) d = dot2(R[2 * g + j][k], q[h][j][k], d);
        x[2 * g + h] = d;
      }
      asm volatile("" : "+v"(x[2 * g]), "+v"(x[2 * g + 1]) :: "memory");
      if constexpr (mode == 0) {
        CAP_WAIT(2);
        R[2 * g] = ldnt(kp + g * 2048 + koff);
        R[2 * g + 1] = ldnt(kp + g * 2048 + 256 + koff);
      } else if constexpr (mode == 3) {
        R[2 * g] = __builtin_amdgcn_raw_buffer_load_b128(krs, koff + g * 2048, (unsigned)tile_off(TN), 2);
        R[2 * g + 1] = __builtin_amdgcn_raw_buffer_load_b128(krs, koff + g * 2048 + 256, (unsigned)tile_off(TN), 2);
      } else if constexpr (mode == 2) {
        CAP_WAIT(2);
        R[2 * g] = ldnt(vp + (2 * g) * 1024 + voff);
        R[2 * g + 1] = ldnt(vp + (2 * g + 1) * 1024 + voff);
      }
      SB();
    }
#define TR_STEP(HALF, CTRL, BIT) _Pragma("unroll") for (int i = 0; i < (HALF); ++i) { \
      const float lo_ = x[i], hi_ = x[i + (HALF)]; const float t1_ = lo_ + dpp<CTRL>(lo_), t2_ = hi_ + dpp<CTRL>(hi_); \
      x[i] = (c & (BIT)) ? t2_ : t1_; }
    TR_STEP(4, XOR8, 8) TR_STEP(2, XOR7, 4) TR_STEP(1, XOR2, 2)
#undef TR_STEP
    float sc = x[0] + dpp<XOR1>(x[0]);
    sc = rbf(sc) * C2;
    mx = fmaxf(mx, sc);
    sp[T * 32 + pw] = sc;
  };
  auto vtile = [&](u32x4 (&R)[8], auto MODE, int T, int TN) {
    constexpr int mode = decltype(MODE)::value;
    const unsigned char* vp = vbase + tile_off(TN);
    f32x4 ppv[4];
#pragma unroll
    for (int i2 = 0; i2 < 4; ++i2) ppv[i2] = *reinterpret_cast<const f32x4*>(sp + T * 32 + r2 * 16 + i2 * 4);
    SB();
#pragma unroll
    for (int i2 = 0; i2 < 4; ++i2) {
      const f32x4 pp = ppv[i2];
#pragma unroll
      for (int e = 0; e < 2; ++e) {
        const int i = 2 * i2 + e;
        const float p0 = pp[2 * e], p1 = pp[2 * e + 1];
#pragma unroll
        for (int k = 0; k < 4; ++k) {
          const f32x2 vv = {lo2f(R[i][k]), hi2f(R[i][k])};
          acc[0][k] = __builtin_elementwise_fma(f32x2{p0, p0}, vv, acc[0][k]);
          acc[1][k] = __builtin_elementwise_fma(f32x2{p1, p1}, vv, acc[1][k]);
        }
        asm volatile("" : "+v"(acc[0][0]), "+v"(acc[0][1]), "+v"(acc[0][2]), "+v"(acc[0][3]), "+v"(acc[1][0]), "+v"(acc[1][1]), "+v"(acc[1][2]), "+v"(acc[1][3]) :: "memory");
        if constexpr (mode == 0) { CAP_WAIT(1); R[i] = ldnt(vp + i * 1024 + voff); }
        SB();
      }
    }
  };
  float* const mls = reinterpret_cast<float*>(smem + L_PL);
  auto k_done = [&]() {
    float pm = fmaxf(mx, dpp<ROR4>(mx)); pm = fmaxf(pm, dpp<XOR8>(pm)); pm = x32_max(x16_max(pm));
    mls[wave * 2 + ((c >> 1) & 1)] = pm;
    asm volatile("s_waitcnt lgkmcnt(0)" ::: "memory");
    if (lane == 0) __hip_atomic_fetch_add(qf + 3, 1u, __ATOMIC_RELAXED, __HIP_MEMORY_SCOPE_WORKGROUP);
  };
  constexpr float KC = 0.7978845608028654f, L2E = 1.4426950408889634f;
  constexpr float K1 = -2.f * KC * L2E, K2 = -2.f * KC * 0.044715f * L2E;
  f32x2 c0[4], c1[4], c2c[4], c3[4], a0c[4], a1c[4];
  float wmax = 0.f, bmax = 0.f;
  auto mlp_constants = [&]() {
    const u16* mwp = reinterpret_cast<const u16*>(smem + L_MW);
#pragma unroll
    for (int i = 0; i < 7; ++i) {
      const int f = (tid & 255) + 256 * i;
      const float w = lo2f(mwp[f]), bb = lo2f(mwp[FF + f]), v = lo2f(mwp[2 * FF + f]);
      c3[i >> 1][i & 1] = K2 * w * w * w; c2c[i >> 1][i & 1] = 3.f * K2 * w * w * bb;
      c1[i >> 1][i & 1] = K1 * w + 3.f * K2 * w * bb * bb; c0[i >> 1][i & 1] = K1 * bb + K2 * bb * bb * bb;
      a1c[i >> 1][i & 1] = w * v; a0c[i >> 1][i & 1] = bb * v;
      wmax = fmaxf(wmax, fabsf(w)); bmax = fmaxf(bmax, fabsf(bb));
    }
    c0[3][1] = c1[3][1] = c2c[3][1] = c3[3][1] = a0c[3][1] = a1c[3][1] = 0.f;
#pragma unroll
    for (int i = 0; i < 4; ++i) asm volatile("" : "+v"(c0[i]), "+v"(c1[i]), "+v"(c2c[i]), "+v"(c3[i]), "+v"(a0c[i]), "+v"(a1c[i]));
    asm volatile("" : "+v"(wmax), "+v"(bmax));
  };
  ktile(R0, IC<3>{}, t0, KH + sw + 2 * NSW);
  ktile(R1, IC<3>{}, t0 + td, KH + sw + 3 * NSW);
  if (!streamer) {
    ktile(R2, IC<1>{}, t0 + 2, 0);
    ktile(R3, IC<1>{}, t0 + 3, 0);
    k_done();
  }
  if (streamer) {
    static_assert(((NTW - KH) / NSW) % 2 == 0 && (VEND / NSW) % 2 == 0, "tiles come in pairs");
    int T = KH + sw + 2 * NSW;
    for (; T + 3 * NSW < NTW; T += 2 * NSW) {
      ktile(R0, IC<0>{}, T, T + 2 * NSW);
      ktile(R1, IC<0>{}, T + NSW, T + 3 * NSW);
    }
    ktile(R0, IC<2>{}, T, sw);
    ktile(R1, IC<2>{}, T + NSW, sw + NSW);
    k_done();
    while (__hip_atomic_load(qf + 4, __ATOMIC_ACQUIRE, __HIP_MEMORY_SCOPE_WORKGROUP) < 4u) {}
    for (T = sw; T + 3 * NSW < VEND; T += 2 * NSW) {
      if (T == VEND - NSW * 2 * (XR + 1) + (NSW - 1)) __hip_atomic_store(qf + 2, 1u, __ATOMIC_RELAXED, __HIP_MEMORY_SCOPE_WORKGROUP);
      vtile(R0, IC<0>{}, T, T + 2 * NSW);
      vtile(R1, IC<0>{}, T + NSW, T + 3 * NSW);
    }
    vtile(R0, IC<1>{}, T, 0);
    vtile(R1, IC<1>{}, T + NSW, 0);
  }
  u32x4 BF[2][8];
  if (wave < 4) {
    if (wave == 3) {
      const u32x4 e4 = {EMPTY, EMPTY, EMPTY, EMPTY};
      __builtin_amdgcn_raw_buffer_store_b128(e4, wsr, prec + lane * 16, 0, SC1);
      __builtin_amdgcn_raw_buffer_store_b128(e4, wsr, prec + 1024 + lane * 16, 0, SC1);
      const unsigned yo = yout + (lane >= 32 ? 8 * DM * 4 : 0) + (lane & 15) * 16;
      __builtin_amdgcn_raw_buffer_store_b128(e4, wsr, lane < 16 ? prec + 2048 + lane * 16 : ((lane < 48 && jq < 28) ? yo : OOB), 0, SC1);
      asm volatile("s_waitcnt vmcnt(0)" ::: "memory");
      __builtin_amdgcn_raw_buffer_store_b64(__builtin_bit_cast(u32x2, epoch), wsr, lane == 0 ? (unsigned)WS_READY + bid * 8 : OOB, 0, SC1);
    }
    const u16* wp = prm.wo + (jq < 28 ? (size_t)(kvh * 512 + 128 * wave + 32 * ((lane >> 3) & 1) + 8 * r) * DM + cq * 64 : (size_t)0) + (lane & 7) * 8;
#pragma unroll
    for (int u = 0; u < 2; ++u) {
      u32x4 W[8];
#pragma unroll
      for (int i = 0; i < 8; ++i) W[i] = ldnt(wp + (64 * u + i) * DM);
#pragma unroll
      for (int p = 0; p < 4; ++p)
#pragma unroll
        for (int k = 0; k < 4; ++k) {
          BF[u][2 * k][p] = __builtin_amdgcn_perm(W[2 * p + 1][k], W[2 * p][k], 0x05040100u);
          BF[u][2 * k + 1][p] = __builtin_amdgcn_perm(W[2 * p + 1][k], W[2 * p][k], 0x07060302u);
        }
#pragma unroll
      for (int j = 0; j < 8; ++j) asm volatile("" : "+v"(BF[u][j]));
      SB();
    }
    {
      const int mi = tid < 224 ? tid : 0;
      const u32x4 m0 = ldnt(prm.w1 + mi * 8), m1 = ldnt(prm.b1 + mi * 8), m2 = ldnt(prm.w2 + mi * 8);
      const unsigned b2r = __builtin_nontemporal_load(prm.b2);
      *reinterpret_cast<unsigned*>(smem + L_EP + 8) = b2r;
      *reinterpret_cast<u32x4*>(smem + L_MW + mi * 16) = m0;
      *reinterpret_cast<u32x4*>(smem + L_MW + FF * 2 + mi * 16) = m1;
      *reinterpret_cast<u32x4*>(smem + L_MW + 2 * FF * 2 + mi * 16) = m2;
    }
    asm volatile("s_waitcnt lgkmcnt(0)" ::: "memory");
    if (lane == 0) __hip_atomic_fetch_add(qf + 1, 1u, __ATOMIC_RELAXED, __HIP_MEMORY_SCOPE_WORKGROUP);
    const unsigned char* rdyp = ws + WS_READY + tid * 8;
    unsigned spins = 0;
    while (!__all(gload(rdyp) == epoch) && ++spins < SPIN_CAP) __builtin_amdgcn_s_sleep(64);
  }
  if (!streamer) {
    while (__hip_atomic_load(qf + 1, __ATOMIC_RELAXED, __HIP_MEMORY_SCOPE_WORKGROUP) < 16u) __builtin_amdgcn_s_sleep(8);
    asm volatile("" ::: "memory");
    mlp_constants();
    if (wave < 4) {
      while (__hip_atomic_load(qf + 3, __ATOMIC_ACQUIRE, __HIP_MEMORY_SCOPE_WORKGROUP) < (unsigned)NWV) __builtin_amdgcn_s_sleep(1);
      f32x4 sv[4], mv[4];
#pragma unroll
      for (int i = 0; i < 4; ++i) { sv[i] = *reinterpret_cast<const f32x4*>(sp + (i * 256 + tid) * 4); mv[i] = *reinterpret_cast<const f32x4*>(mls + i * 4); }
      const float M0 = fmaxf(fmaxf(fmaxf(mv[0][0], mv[0][2]), fmaxf(mv[1][0], mv[1][2])), fmaxf(fmaxf(mv[2][0], mv[2][2]), fmaxf(mv[3][0], mv[3][2])));
      const float M1 = fmaxf(fmaxf(fmaxf(mv[0][1], mv[0][3]), fmaxf(mv[1][1], mv[1][3])), fmaxf(fmaxf(mv[2][1], mv[2][3]), fmaxf(mv[3][1], mv[3][3])));
      float l0 = 0.f, l1 = 0.f;
#pragma unroll
      for (int i = 0; i < 4; ++i) {
        const f32x4 p = {__builtin_amdgcn_exp2f(sv[i][0] - M0), __builtin_amdgcn_exp2f(sv[i][1] - M1), __builtin_amdgcn_exp2f(sv[i][2] - M0), __builtin_amdgcn_exp2f(sv[i][3] - M1)};
        *reinterpret_cast<f32x4*>(sp + (i * 256 + tid) * 4) = p;
        l0 += p[0] + p[2]; l1 += p[1] + p[3];
      }
      asm volatile("s_waitcnt lgkmcnt(0)" ::: "memory");
      if (lane == 0) __hip_atomic_fetch_add(qf + 4, 1u, __ATOMIC_RELEASE, __HIP_MEMORY_SCOPE_WORKGROUP);
      wave_sum2(l0, l1);
      *reinterpret_cast<f32x2*>(mls + 16 + wave * 2) = f32x2{l0, l1};
    }
    while (__hip_atomic_load(qf + 2, __ATOMIC_ACQUIRE, __HIP_MEMORY_SCOPE_WORKGROUP) == 0u) __builtin_amdgcn_s_sleep(4);
    vload(R0, VEND + 2 * wave);
    vload(R1, VEND + 2 * wave + 1);
    vtile(R0, IC<1>{}, VEND + 2 * wave, 0);
    vtile(R1, IC<1>{}, VEND + 2 * wave + 1, 0);
  }
  float* mrg = reinterpret_cast<float*>(smem + L_MRG);
  {
    float* rec = mrg + (wave * 2 + r2) * 260;
    float a8[8];
#pragma unroll
    for (int k = 0; k < 8; ++k) {
      const auto t = __builtin_amdgcn_permlane32_swap(__float_as_uint(acc[0][k >> 1][k & 1]), __float_as_uint(acc[1][k >> 1][k & 1]), false, false);
      a8[k] = __uint_as_float(t[0]) + __uint_as_float(t[1]);
    }
    *reinterpret_cast<f32x4*>(rec + 4 + c2 * 8) = f32x4{a8[0], a8[1], a8[2], a8[3]};
    *reinterpret_cast<f32x4*>(rec + 8 + c2 * 8) = f32x4{a8[4], a8[5], a8[6], a8[7]};
  }
  unsigned char* const tl = smem + L_T0;
  __syncthreads();
  if (wave < 2) {
    f32x4 pa[NWV];
#pragma unroll
    for (int w = 0; w < NWV; ++w) pa[w] = *reinterpret_cast<const f32x4*>(mrg + (w * 2 + wave) * 260 + 4 + lane * 4);
    float M = mls[wave];
#pragma unroll
    for (int w = 1; w < NWV; ++w) M = fmaxf(M, mls[w * 2 + wave]);
    const float L = (mls[16 + wave] + mls[18 + wave]) + (mls[20 + wave] + mls[22 + wave]);
    const f32x4 on = (((pa[0] + pa[1]) + (pa[2] + pa[3])) + ((pa[4] + pa[5]) + (pa[6] + pa[7]))) * __builtin_amdgcn_rcpf(L);
    const float lse = M + __builtin_amdgcn_logf(L);
    const u32x4 ob = {pubbits(on[0]), pubbits(on[1]), pubbits(on[2]), pubbits(on[3])};
    __builtin_amdgcn_raw_buffer_store_b128(ob, wsr, prec + tid * 16, 0, SC1);
    __builtin_amdgcn_raw_buffer_store_b32(pubbits(lse), wsr, lane == 0 ? prec + 2048 + wave * 128 : OOB, 0, SC1);
  }
  if (bid == 0 && tid == 448) gstore(ws + WS_EPOCH, epoch);
  if (jq < 28) {
    if (wave >= 4) {
      const int beta = (wave >> 1) & 1, h = wave & 1;
      u32x4 sa[4]; unsigned sl[4];
      unsigned spins = 0;
      for (;;) {
        asm volatile("" ::: "memory");
#pragma unroll
        for (int t = 0; t < 4; ++t) {
          const unsigned rec = (unsigned)WS_P + (unsigned)(t * 64 + (2 * bg + beta) * 8 + kvh) * PREC * 4;
          sa[t] = __builtin_amdgcn_raw_buffer_load_b128(wsr, h * 1024 + lane * 16, rec, SC1);
          sl[t] = __builtin_amdgcn_raw_buffer_load_b32(wsr, 2048 + h * 128, rec, SC1);
        }
        bool ok = true;
#pragma unroll
        for (int t = 0; t < 4; ++t) ok &= full4(sa[t]) && sl[t] != EMPTY;
        if (__all(ok)) break;
        __builtin_amdgcn_s_sleep(SPIN_SLEEP);
        if (++spins > SPIN_CAP) break;
      }
      const float l0 = __uint_as_float(sl[0]), l1 = __uint_as_float(sl[1]), l2 = __uint_as_float(sl[2]), l3 = __uint_as_float(sl[3]);
      const float MM = fmaxf(fmaxf(l0, l1), fmaxf(l2, l3));
      const float e0 = __builtin_amdgcn_exp2f(l0 - MM), e1 = __builtin_amdgcn_exp2f(l1 - MM), e2 = __builtin_amdgcn_exp2f(l2 - MM), e3 = __builtin_amdgcn_exp2f(l3 - MM);
      const float inv = __builtin_amdgcn_rcpf((e0 + e1) + (e2 + e3));
      const f32x4 oo = (e0 * __builtin_bit_cast(f32x4, sa[0]) + e1 * __builtin_bit_cast(f32x4, sa[1]) + e2 * __builtin_bit_cast(f32x4, sa[2]) + e3 * __builtin_bit_cast(f32x4, sa[3])) * inv;
      *reinterpret_cast<u32x2*>(tl + T_OB + (tid & 255) * 8) = u32x2{bfbits(oo[0]) | (bfbits(oo[1]) << 16), bfbits(oo[2]) | (bfbits(oo[3]) << 16)};
    }
    __syncthreads();
    float* red = reinterpret_cast<float*>(tl + T_RED2);
    if (wave < 4) {
      const bf16x8 a0 = *reinterpret_cast<const bf16x8*>(tl + T_OB + (((c >> 1) & 1) * 512 + 128 * wave + 32 * (c & 1) + 8 * r) * 2);
      const bf16x8 a1 = *reinterpret_cast<const bf16x8*>(tl + T_OB + (((c >> 1) & 1) * 512 + 128 * wave + 64 + 32 * (c & 1) + 8 * r) * 2);
      f32x4 y0[2], y1[2];
#pragma unroll
      for (int j = 0; j < 8; ++j) {
        f32x4 d = __builtin_amdgcn_mfma_f32_16x16x32_bf16(a0, __builtin_bit_cast(bf16x8, BF[0][j]), f32x4{0.f, 0.f, 0.f, 0.f}, 0, 0, 0);
        d = __builtin_amdgcn_mfma_f32_16x16x32_bf16(a1, __builtin_bit_cast(bf16x8, BF[1][j]), d, 0, 0, 0);
        y0[j >> 2][j & 3] = (c & 8) ? d[1] : d[0];
        y1[j >> 2][j & 3] = (c & 8) ? d[3] : d[2];
      }
      float* o = red + (wave * 2 + (c >> 3)) * 128 + (c & 7) * 8;
      *reinterpret_cast<f32x4*>(o) = y0[0]; *reinterpret_cast<f32x4*>(o + 4) = y0[1];
      *reinterpret_cast<f32x4*>(o + 64) = y1[0]; *reinterpret_cast<f32x4*>(o + 68) = y1[1];
    }
    __syncthreads();
    if (wave == 7) {
      f32x4 t = *reinterpret_cast<const f32x4*>(red + (r2 * 4) * 128 + c2 * 4);
#pragma unroll
      for (int w = 1; w < 4; ++w) t += *reinterpret_cast<const f32x4*>(red + (r2 * 4 + w) * 128 + c2 * 4);
      const u32x4 yb = {pubbits(x32_sum(t[0])), pubbits(x32_sum(t[1])), pubbits(x32_sum(t[2])), pubbits(x32_sum(t[3]))};
      __builtin_amdgcn_raw_buffer_store_b128(yb, wsr, lane < 32 ? yout + (lane >> 4) * (8 * DM * 4) + c * 16 : OOB, 0, SC1);
    }
  }
  if (streamer) mlp_constants();
  float* hs = reinterpret_cast<float*>(tl + T_HS);
  float hv = 0.f;
  if (wave < 7) {
    const float xres = lo2f(reinterpret_cast<const u16*>(smem + L_XR)[tid]);
    const unsigned yin = (unsigned)WS_Y + (unsigned)b * (8 * DM * 4);
    unsigned yv[8];
    unsigned spins = 0;
    for (;;) {
      asm volatile("" ::: "memory");
#pragma unroll
      for (int k = 0; k < 8; ++k) yv[k] = __builtin_amdgcn_raw_buffer_load_b32(wsr, tid * 4 + k * (DM * 4), yin, SC1);
      unsigned mx = yv[0];
#pragma unroll
      for (int k = 1; k < 8; ++k) mx = max(mx, yv[k]);
      if (__all(mx != EMPTY)) break;
      __builtin_amdgcn_s_sleep(SPIN_SLEEP);
      if (++spins > SPIN_CAP) break;
    }
    const float t = ((__uint_as_float(yv[0]) + __uint_as_float(yv[1])) + (__uint_as_float(yv[2]) + __uint_as_float(yv[3]))) +
                    ((__uint_as_float(yv[4]) + __uint_as_float(yv[5])) + (__uint_as_float(yv[6]) + __uint_as_float(yv[7])));
    hv = rbf(xres + rbf(t));
    hs[tid] = hv;
  }
  const int jo = kvh * 4 + s, par = wave >> 2;
  float mbs[2], rds[2];
  {
    const float cw = __builtin_bit_cast(float, __builtin_amdgcn_readfirstlane(__builtin_bit_cast(int, hv)));
    const float dv = hv - cw;
    float s1 = dv, s2 = dv * dv;
    wave_sum2(s1, s2);
    f32x2* stp = reinterpret_cast<f32x2*>(tl + T_ST);
    stp[wave] = f32x2{fmaf(s1, 1.f / 64.f, cw), fmaf(-s1 * (1.f / 64.f), s1, s2)};
    __syncthreads();
    f32x2 sv[7];
#pragma unroll
    for (int w = 0; w < 7; ++w) sv[w] = stp[w];
    const f32x2 tot = ((sv[0] + sv[1]) + (sv[2] + sv[3])) + ((sv[4] + sv[5]) + sv[6]);
    const float mean = tot[0] * (1.f / 7.f);
    float dm0 = 0.f, dm1 = 0.f;
#pragma unroll
    for (int w = 0; w < 7; ++w) { const float d = sv[w][0] - mean; if (w & 1) dm1 = fmaf(d, d, dm1); else dm0 = fmaf(d, d, dm0); }
    mbs[0] = rbf(mean); rds[0] = ln_rden(fmaf(64.f, dm0 + dm1, tot[1]) * (1.f / DM));
    hv = wave < 7 ? rbf(rbf(hv - mbs[0]) * rds[0]) : 0.f;
  }
  {
    float s1 = hv, s2 = hv * hv;
    wave_sum2(s1, s2);
    f32x2* stp = reinterpret_cast<f32x2*>(tl + T_ST + 64);
    stp[wave] = f32x2{s1, s2};
    __syncthreads();
    f32x2 sv[7];
#pragma unroll
    for (int w = 0; w < 7; ++w) sv[w] = stp[w];
    const f32x2 tot = (((sv[0] + sv[1]) + (sv[2] + sv[3])) + ((sv[4] + sv[5]) + sv[6])) * (1.f / DM);
    mbs[1] = rbf(tot[0]); rds[1] = ln_rden(fmaxf(fmaf(-tot[0], tot[0], tot[1]), 0.f));
  }
  float g1 = hs[14 * jo + 2 * (lane < 7 ? lane : 6) + par];
  g1 = rbf(rbf(g1 - mbs[0]) * rds[0]);
  g1 = rbf(rbf(g1 - mbs[1]) * rds[1]);
  const float g2 = g1 * g1, g3 = g2 * g1;
  float ma[7];
  auto mlp = [&](auto SAFE) {
    constexpr bool safe = decltype(SAFE)::value != 0;
#pragma unroll
    for (int mo = 0; mo < 7; ++mo) {
      const float h1 = __builtin_bit_cast(float, __builtin_amdgcn_readlane(__builtin_bit_cast(int, g1), mo));
      const float h2 = __builtin_bit_cast(float, __builtin_amdgcn_readlane(__builtin_bit_cast(int, g2), mo));
      const float h3 = __builtin_bit_cast(float, __builtin_amdgcn_readlane(__builtin_bit_cast(int, g3), mo));
      const f32x2 H1 = {h1, h1}, H2 = {h2, h2}, H3 = {h3, h3};
      f32x2 d[4], a[4];
#pragma unroll
      for (int i = 0; i < 4; ++i) {
        const f32x2 z = __builtin_elementwise_fma(c3[i], H3, __builtin_elementwise_fma(c2c[i], H2, __builtin_elementwise_fma(c1[i], H1, c0[i])));
        a[i] = __builtin_elementwise_fma(a1c[i], H1, a0c[i]);
        d[i][0] = __builtin_amdgcn_exp2f(z[0]);
        d[i][1] = i < 3 ? __builtin_amdgcn_exp2f(z[1]) : 0.f;
        d[i] += 1.f;
      }
      float acc1 = a[3][0] * __builtin_amdgcn_rcpf(d[3][0]);
#pragma unroll
      for (int i = 0; i < 3; ++i) {
        if constexpr (safe) { acc1 = fmaf(a[i][0], __builtin_amdgcn_rcpf(d[i][0]), acc1); acc1 = fmaf(a[i][1], __builtin_amdgcn_rcpf(d[i][1]), acc1); }
        else acc1 = fmaf(fmaf(a[i][0], d[i][1], a[i][1] * d[i][0]), __builtin_amdgcn_rcpf(d[i][0] * d[i][1]), acc1);
      }
      ma[mo] = acc1;
    }
  };
  {
    float hm = fabsf(g1);
    hm = fmaxf(hm, dpp<XOR1>(hm)); hm = fmaxf(hm, dpp<XOR2>(hm)); hm = fmaxf(hm, dpp<XOR7>(hm));
    hm = __builtin_bit_cast(float, __builtin_amdgcn_readfirstlane(__builtin_bit_cast(int, hm)));
    if (__all(fmaf(hm, wmax, bmax) <= 7.5f)) mlp(IC<0>{}); else mlp(IC<1>{});
  }
  {
    float x[8] = {ma[0], ma[1], ma[2], ma[3], ma[4], ma[5], ma[6], 0.f};
#define TR_STEP(HALF, CTRL, BIT) _Pragma("unroll") for (int i = 0; i < (HALF); ++i) { \
      const float lo_ = x[i], hi_ = x[i + (HALF)]; const float t1_ = lo_ + dpp<CTRL>(lo_), t2_ = hi_ + dpp<CTRL>(hi_); \
      x[i] = (c & (BIT)) ? t2_ : t1_; }
    TR_STEP(4, XOR8, 8) TR_STEP(2, XOR7, 4) TR_STEP(1, XOR2, 2)
#undef TR_STEP
    float t = x[0] + dpp<XOR1>(x[0]);
    t = x32_sum(x16_sum(t));
    float* red3 = reinterpret_cast<float*>(tl + T_RED3);
    red3[wave * 8 + (c >> 1)] = t;
    __syncthreads();
    if (wave == 0) {
      const int to = lane < 14 ? lane : 13;
      const int mo = to >> 1, pr = to & 1;
      const float y = (red3[(pr * 4 + 0) * 8 + mo] + red3[(pr * 4 + 1) * 8 + mo]) + (red3[(pr * 4 + 2) * 8 + mo] + red3[(pr * 4 + 3) * 8 + mo]);
      const int d = 14 * jo + to;
      const float y2 = rbf(rbf(y) + lo2f(*reinterpret_cast<const unsigned*>(smem + L_EP + 8)));
      const u16 ov = (u16)bfbits(hs[d] + y2);
      if (lane < 14) prm.out[b * DM + d] = ov;
    }
  }
}
}

extern "C" void kernel_launch(void* const* d_in, const int* in_sizes, int n_in, void* d_out, int out_size, void* d_ws, size_t ws_size,
                              hipStream_t stream) {
  Params p;
  p.x = (const u16*)d_in[0]; p.kc = (const u16*)d_in[1]; p.vc = (const u16*)d_in[2]; p.wq = (const u16*)d_in[3]; p.wo = (const u16*)d_in[4];
  p.w1 = (const u16*)d_in[6]; p.b1 = (const u16*)d_in[7]; p.w2 = (const u16*)d_in[8]; p.b2 = (const u16*)d_in[9];
  p.out = (u16*)d_out; p.ws = (unsigned char*)d_ws;
  k_fused<<<NWG, NT, 0, stream>>>(p);
}
```
